```python
import torch

def _softmax(x, axis=-1):
    return torch.softmax(x, dim=axis)
def _gelu(x):
    return torch.nn.functional.gelu(x, approximate="tanh")
def _silu(x):
    return torch.nn.functional.silu(x)
def _sigmoid(x):
    return torch.sigmoid(x)
def _layernorm(x, axis=-1, eps=1e-5):
    m = x.mean(dim=axis, keepdim=True)
    v = x.var(dim=axis, keepdim=True, unbiased=False)
    return (x - m) / torch.sqrt(v + eps)
def _scatter_add(tgt, idx, src, axis=0):
    return tgt.clone().index_add_(axis, idx, src)
def _segment_sum(src, seg, n):
    out = torch.zeros((n,) + src.shape[1:], dtype=src.dtype)
    return out.index_add_(0, seg, src)
def _pad(x, pw):
    flat = tuple(v for pair in reversed(pw) for v in pair)
    return torch.nn.functional.pad(x, flat)

kernel_name = 'standalone__gqa_block_cached__near_ridge'

def setup_inputs():

    _w = (lambda a: (lambda t: t.to(torch.bfloat16) if t.is_floating_point() else t)(torch.as_tensor(a)))
    B, Sq, D = 16, 64, 448
    Hq, Hkv, Sk, Dh = 16, 8, 512, 256
    assert Hq % Hkv == 0, (Hq, Hkv)
    return {
        "x": _w((lambda *s: torch.randn(*s, dtype=torch.bfloat16) * 0.8)(B, Sq, D)),
        "k_cache": _w((lambda *s: torch.randn(*s, dtype=torch.bfloat16) * 0.8)(B, Hkv, Sk, Dh)),
        "v_cache": _w((lambda *s: torch.randn(*s, dtype=torch.bfloat16) * 0.05)(B, Hkv, Sk, Dh)),
        "wq": _w((lambda *s: torch.randn(*s, dtype=torch.bfloat16) * 0.8)(D, Hq * Dh) * (D ** -0.5)),
        "wo": _w((lambda *s: torch.randn(*s, dtype=torch.bfloat16) * 0.05)(Hq * Dh, D) * ((Hq * Dh) ** -0.5)),
    }

def reference(x, k_cache, v_cache, wq, wo):
    B, Sq, D = x.shape
    Hq, Dh = 16, 256
    Hkv = k_cache.shape[1]
    rep = Hq // Hkv
    q = (lambda x, *d: x.permute(*d))((x @ wq).reshape(B, Sq, Hq, Dh), 0, 2, 1, 3)
    k = (lambda x, n, axis: torch.repeat_interleave(x, n, dim=axis))(k_cache, rep, axis=1)
    v = (lambda x, n, axis: torch.repeat_interleave(x, n, dim=axis))(v_cache, rep, axis=1)
    scores = torch.einsum("bhqd,bhkd->bhqk", q, k) * (Dh ** -0.5)
    p = _softmax(scores, axis=-1)
    o = torch.einsum("bhqk,bhkd->bhqd", p, v)
    return (lambda x, *d: x.permute(*d))(o, 0, 2, 1, 3).reshape(B, Sq, Hq * Dh) @ wo
```

```cpp
#include <hip/hip_runtime.h>
#include <hip/hip_bf16.h>
typedef __hip_bfloat16 bf16;
constexpr int B=16,SQ=64,D=448,HQ=16,HKV=8,SK=512,DH=256;
__global__ void qproj(const bf16* x,const bf16* wq,bf16* q){
  int row=blockIdx.x;
  for(int c=threadIdx.x;c<HQ*DH;c+=blockDim.x){
    float a=0; for(int k=0;k<D;k++) a+=__bfloat162float(x[row*D+k])*__bfloat162float(wq[k*HQ*DH+c]);
    q[row*HQ*DH+c]=__float2bfloat16(a);
  }
}
__global__ void attn(const bf16* q,const bf16* k,const bf16* v,bf16* o){
  int row=blockIdx.x, h=blockIdx.y; int b=row/SQ; int hk=h/2;
  __shared__ float s[SK]; __shared__ float qs[DH]; __shared__ float red[256];
  int t=threadIdx.x;
  qs[t]=__bfloat162float(q[row*HQ*DH+h*DH+t]); __syncthreads();
  const bf16* kb=k+((size_t)(b*HKV+hk))*SK*DH; const bf16* vb=v+((size_t)(b*HKV+hk))*SK*DH;
  float m=-1e30f;
  for(int j=t;j<SK;j+=256){ float a=0; for(int d=0;d<DH;d++) a+=qs[d]*__bfloat162float(kb[j*DH+d]); a*=0.0625f; s[j]=a; m=fmaxf(m,a);}
  red[t]=m; __syncthreads(); for(int st=128;st>0;st>>=1){ if(t<st) red[t]=fmaxf(red[t],red[t+st]); __syncthreads(); }
  m=red[0]; __syncthreads(); float l=0;
  for(int j=t;j<SK;j+=256){ float e=__expf(s[j]-m); s[j]=e; l+=e; }
  red[t]=l; __syncthreads(); for(int st=128;st>0;st>>=1){ if(t<st) red[t]+=red[t+st]; __syncthreads(); }
  l=red[0];
  float a=0; for(int j=0;j<SK;j++) a+=s[j]*__bfloat162float(vb[j*DH+t]);
  o[row*HQ*DH+h*DH+t]=__float2bfloat16(a/l);
}
__global__ void oproj(const bf16* o,const bf16* wo,bf16* out){
  int row=blockIdx.x;
  for(int c=threadIdx.x;c<D;c+=blockDim.x){
    float a=0; for(int k=0;k<HQ*DH;k++) a+=__bfloat162float(o[row*HQ*DH+k])*__bfloat162float(wo[k*D+c]);
    out[row*D+c]=__float2bfloat16(a);
  }
}
extern "C" void kernel_launch(void* const* d_in, const int* in_sizes, int n_in, void* d_out, int out_size, void* d_ws, size_t ws_size, hipStream_t stream) {
  const bf16* x=(const bf16*)d_in[0]; const bf16* k=(const bf16*)d_in[1]; const bf16* v=(const bf16*)d_in[2];
  const bf16* wq=(const bf16*)d_in[3]; const bf16* wo=(const bf16*)d_in[4];
  bf16* q=(bf16*)d_ws; bf16* o=q+(size_t)B*SQ*HQ*DH;
  qproj<<<B*SQ,256,0,stream>>>(x,wq,q);
  attn<<<dim3(B*SQ,HQ),256,0,stream>>>(q,k,v,o);
  oproj<<<B*SQ,256,0,stream>>>(o,wo,(bf16*)d_out);
}
```

```cpp
#include <hip/hip_runtime.h>
#include <hip/hip_bf16.h>
#include <utility>
template<int... Is, class F> __device__ __forceinline__ void sfor_seq(std::integer_sequence<int,Is...>, F&& f){ (f(std::integral_constant<int,Is>{}), ...); }
template<int N, class F> __device__ __forceinline__ void sfor(F&& f){ sfor_seq(std::make_integer_sequence<int,N>{}, f); }
template<int N> __device__ __forceinline__ void vmwait(){ asm volatile("s_waitcnt vmcnt(%0)"::"i"(N):"memory"); }
typedef unsigned v4u __attribute__((ext_vector_type(4)));
#define GAS __attribute__((address_space(1)))
#define LAS __attribute__((address_space(3)))
__device__ __forceinline__ unsigned long long rt() { return __builtin_amdgcn_s_memrealtime(); }
struct Sch { int n; int kind[80]; int idx[80]; int mark; };
constexpr Sch mk(int S, int G){
  Sch s{}; int n=0;
  auto add=[&](int k,int i){ s.kind[n]=k; s.idx[n]=i; n++; };
  auto grp=[&](int k,int g){ for(int i=0;i<G;i++) add(k, g*G+i); };
  int NG = 8/G;
  if (S==0 || S==1) {
    for(int i=0;i<4;i++) add(0,i);
    for(int i=0;i<14;i++) add(1,i);
    s.mark = n;
    if (S==0) { for(int i=0;i<8;i++){ add(2,i); add(3,i);} for(int i=0;i<8;i++){ add(4,i); add(5,i);} }
    else {
      grp(2,0);
      for(int g=1; g<2*NG; g++){ if (g<NG) grp(2,g); else grp(4,g-NG); if (g-1<NG) grp(3,g-1); else grp(5,g-1-NG);}
      grp(5,NG-1);
    }
    for(int i=0;i<14;i++) add(6,i);
  } else if (S>=2) {
    int P = (S==2)? 6 : (S==3)? 4 : (S==4) ? 3 : 5;
    for(int i=0;i<4;i++) add(0,i);
    int og=0;
    auto own=[&](){ if (og<NG) grp(2,og); else grp(4,og-NG); og++; };
    int xg=0; auto oth=[&](){ if (xg<NG) grp(3,xg); else grp(5,xg-NG); xg++; };
    for(int i=0;i<14;i++){ if (i%P==2%P) own(); add(1,i);}
    s.mark = n;
    while (og<2*NG || xg<2*NG) { if (xg<2*NG) oth(); if (og<2*NG) own(); }
    for(int i=0;i<14;i++) add(6,i);
  }
  s.n=n; return s;
}
template<int S, int G, int PD, int ROT, int SWAP, int AW, int AO, int AX>
__global__ __launch_bounds__(512) void k(const char* __restrict__ X, const char* __restrict__ K, const char* __restrict__ V, const char* __restrict__ WQ, const char* __restrict__ WO, unsigned long long* ts){
  extern __shared__ __attribute__((aligned(16))) char lds[];
  unsigned long long t0 = rt();
  const int bid = blockIdx.x, tid = threadIdx.x, w = __builtin_amdgcn_readfirstlane(tid>>6), l = tid&63;
  const int xcd = bid & 7, r = bid >> 3, b = r >> 1, role = r & 1, h = xcd*2 + role, kv = b*8 + xcd;
  constexpr Sch sch = mk(S,G);
  const int rot = ROT ? (b*14)>>4 : 0;
  const int ro = SWAP ? role : 0;
  const char* kb = K + (size_t)kv*262144 + w*2048 + l*16;
  const char* vb = V + (size_t)kv*262144 + w*2048 + l*16;
  const char* xb = X + (size_t)b*57344 + w*2048 + l*16;
  const char* qb = WQ + h*512 + (size_t)(w*4 + (l>>5))*8192 + (l&31)*16;
  const char* ob = WO + (size_t)h*229376 + w*2048 + l*16;
  unsigned long long tq=0, tv=0;
  sfor<sch.n + PD>([&](auto ic){ constexpr int i = decltype(ic)::value;
    if constexpr (i >= PD) { constexpr int rem = (i < sch.n ? PD-1 : sch.n + PD - 1 - i); vmwait<rem*2>(); __builtin_amdgcn_s_barrier();
      if constexpr (i-PD == sch.mark-1) tq = rt();
      if constexpr (i-PD == sch.n-15) tv = rt(); }
    if constexpr (i < sch.n) {
      constexpr int kind = sch.kind[i], idx = sch.idx[i];
      const char* p; int st = 1024;
      if constexpr (kind==0) { int c = SWAP ? (idx + 2*role)&3 : idx; p = xb + c*14336; if (w==7) p -= 2048; }
      else if constexpr (kind==1) { int j = idx + rot; if (j>=14) j-=14; p = qb + (size_t)j*32*8192; st = 2*8192; }
      else if constexpr (kind==2) p = kb + ((idx/G)*2*G + ro*G + idx%G)*16384;
      else if constexpr (kind==3) p = kb + ((idx/G)*2*G + (1-ro)*G + idx%G)*16384;
      else if constexpr (kind==4) p = vb + ((idx/G)*2*G + ro*G + idx%G)*16384;
      else if constexpr (kind==5) p = vb + ((idx/G)*2*G + (1-ro)*G + idx%G)*16384;
      else { int j = idx + rot; if (j>=14) j-=14; p = ob + j*16384; }
      LAS char* dst = (LAS char*)lds + (i%10)*16384 + w*2048;
      constexpr int aux = (kind==2||kind==4) ? AO : (kind==3||kind==5) ? AX : AW;
      __builtin_amdgcn_global_load_lds((const GAS void*)p, (LAS void*)dst, 16, 0, aux);
      __builtin_amdgcn_global_load_lds((const GAS void*)(p+st), (LAS void*)(dst+1024), 16, 0, aux);
    }
    __builtin_amdgcn_sched_barrier(0);
  });
  unsigned long long t1 = rt();
  if (tid==0){ ts[bid*4]=t0; ts[bid*4+1]=t1; ts[bid*4+2]=tq; ts[bid*4+3]=tv; }
}


__global__ void delayk(const unsigned long long* ts, int which){
  __shared__ unsigned long long mn[256], mx[256];
  int t = threadIdx.x; mn[t] = ts[4*t]; mx[t] = ts[4*t+which]; __syncthreads();
  if (t==0){ unsigned long long a=~0ull,b=0; for(int i=0;i<256;i++){ a = mn[i]<a?mn[i]:a; b = mx[i]>b?mx[i]:b; }
    unsigned long long span = b-a; if (span > 10000) span = 10000; unsigned long long t0 = rt(); while (rt() - t0 < span*0) __builtin_amdgcn_s_sleep(8); }
}
typedef __hip_bfloat16 bf16;
constexpr int B=16,SQ=64,D=448,HQ=16,HKV=8,SK=512,DH=256;
__global__ void qproj(const bf16* x,const bf16* wq,bf16* q){
  int row=blockIdx.x;
  for(int c=threadIdx.x;c<HQ*DH;c+=blockDim.x){
    float a=0; for(int k=0;k<D;k++) a+=__bfloat162float(x[row*D+k])*__bfloat162float(wq[k*HQ*DH+c]);
    q[row*HQ*DH+c]=__float2bfloat16(a);
  }
}
__global__ void attn(const bf16* q,const bf16* k,const bf16* v,bf16* o){
  int row=blockIdx.x, h=blockIdx.y; int b=row/SQ; int hk=h/2;
  __shared__ float s[SK]; __shared__ float qs[DH]; __shared__ float red[256];
  int t=threadIdx.x;
  qs[t]=__bfloat162float(q[row*HQ*DH+h*DH+t]); __syncthreads();
  const bf16* kb=k+((size_t)(b*HKV+hk))*SK*DH; const bf16* vb=v+((size_t)(b*HKV+hk))*SK*DH;
  float m=-1e30f;
  for(int j=t;j<SK;j+=256){ float a=0; for(int d=0;d<DH;d++) a+=qs[d]*__bfloat162float(kb[j*DH+d]); a*=0.0625f; s[j]=a; m=fmaxf(m,a);}
  red[t]=m; __syncthreads(); for(int st=128;st>0;st>>=1){ if(t<st) red[t]=fmaxf(red[t],red[t+st]); __syncthreads(); }
  m=red[0]; __syncthreads(); float l=0;
  for(int j=t;j<SK;j+=256){ float e=__expf(s[j]-m); s[j]=e; l+=e; }
  red[t]=l; __syncthreads(); for(int st=128;st>0;st>>=1){ if(t<st) red[t]+=red[t+st]; __syncthreads(); }
  l=red[0];
  float a=0; for(int j=0;j<SK;j++) a+=s[j]*__bfloat162float(vb[j*DH+t]);
  o[row*HQ*DH+h*DH+t]=__float2bfloat16(a/l);
}
__global__ void oproj(const bf16* o,const bf16* wo,bf16* out){
  int row=blockIdx.x;
  for(int c=threadIdx.x;c<D;c+=blockDim.x){
    float a=0; for(int k=0;k<HQ*DH;k++) a+=__bfloat162float(o[row*HQ*DH+k])*__bfloat162float(wo[k*D+c]);
    out[row*D+c]=__float2bfloat16(a);
  }
}
extern "C" void kernel_launch(void* const* d_in, const int* in_sizes, int n_in, void* d_out, int out_size, void* d_ws, size_t ws_size, hipStream_t stream) {
  const bf16* x=(const bf16*)d_in[0]; const bf16* kk=(const bf16*)d_in[1]; const bf16* v=(const bf16*)d_in[2];
  const bf16* wq=(const bf16*)d_in[3]; const bf16* wo=(const bf16*)d_in[4];
  bf16* q=(bf16*)d_ws; bf16* o=q+(size_t)B*SQ*HQ*DH;
  { unsigned long long* ts = (unsigned long long*)((char*)d_ws + (64<<20));
    (void)hipFuncSetAttribute((const void*)k<0,1,4,1,0,0,0,0>, hipFuncAttributeMaxDynamicSharedMemorySize, 160*1024);
    k<0,1,4,1,0,0,0,0><<<256,512,160*1024,stream>>>((const char*)x,(const char*)kk,(const char*)v,(const char*)wq,(const char*)wo,ts);
    delayk<<<1,256,0,stream>>>(ts,1); }
  qproj<<<B*SQ,256,0,stream>>>(x,wq,q);
  attn<<<dim3(B*SQ,HQ),256,0,stream>>>(q,kk,v,o);
  oproj<<<B*SQ,256,0,stream>>>(o,wo,(bf16*)d_out);
}
```

```cpp
#include <hip/hip_runtime.h>
#include <hip/hip_bf16.h>
#include <utility>

#define GAS __attribute__((address_space(1)))
#define LAS __attribute__((address_space(3)))
#define DI __device__ __forceinline__
typedef short bf16x8 __attribute__((ext_vector_type(8)));
typedef short s16x4 __attribute__((ext_vector_type(4)));
typedef float f32x4 __attribute__((ext_vector_type(4)));
typedef float f32x2 __attribute__((ext_vector_type(2)));
typedef float f32x16 __attribute__((ext_vector_type(16)));
typedef unsigned u32x4 __attribute__((ext_vector_type(4)));
typedef unsigned u32x2 __attribute__((ext_vector_type(2)));
typedef __bf16 bf16x2v __attribute__((ext_vector_type(2)));

template <int... Is, class F> DI void sfor_seq(std::integer_sequence<int, Is...>, F&& f) { (f(std::integral_constant<int, Is>{}), ...); }
template <int N, class F> DI void sfor(F&& f) { sfor_seq(std::make_integer_sequence<int, N>{}, f); }
template <int N> DI void vmwait() { asm volatile("s_waitcnt vmcnt(%0)" ::"i"(N) : "memory"); }
DI void lgkm0() { asm volatile("s_waitcnt lgkmcnt(0)" ::: "memory"); }
DI void bar() { asm volatile("s_waitcnt lgkmcnt(0)\n\ts_barrier" ::: "memory"); }
DI unsigned pk2(float a, float b) { f32x2 v = {a, b}; return __builtin_bit_cast(unsigned, __builtin_convertvector(v, bf16x2v)); }
DI bf16x8 lds128(LAS char* p) { return *(LAS bf16x8*)p; }
DI bf16x8 ldstr(LAS char* p0, LAS char* p1) {
  s16x4 a = __builtin_amdgcn_ds_read_tr16_b64_v4i16((LAS s16x4*)p0);
  s16x4 b = __builtin_amdgcn_ds_read_tr16_b64_v4i16((LAS s16x4*)p1);
  return __builtin_shufflevector(a, b, 0, 1, 2, 3, 4, 5, 6, 7);
}
template <int NT> DI void dma16(u32x4 rs, unsigned ldsaddr, int voff, int soff) {
  if constexpr (NT) asm volatile("s_mov_b32 m0, %0\n\ts_nop 0\n\tbuffer_load_dwordx4 %1, %2, %3 offen nt lds" ::"s"(ldsaddr), "v"(voff), "s"(rs), "s"(soff) : "memory");
  else asm volatile("s_mov_b32 m0, %0\n\ts_nop 0\n\tbuffer_load_dwordx4 %1, %2, %3 offen lds" ::"s"(ldsaddr), "v"(voff), "s"(rs), "s"(soff) : "memory");
}
DI u32x4 mk_rsrc(const char* p, unsigned bytes) {
  unsigned long long a = (unsigned long long)p;
  u32x4 r = {(unsigned)a, (unsigned)(a >> 32) & 0xffffu, bytes, 0x00020000u};
  return r;
}
#define MFMA32(a, b, c) __builtin_amdgcn_mfma_f32_32x32x16_bf16((a), (b), (c), 0, 0, 0)
#define MFMA16(a, b, c) __builtin_amdgcn_mfma_f32_16x16x32_bf16((a), (b), (c), 0, 0, 0)

constexpr int NB = 16, SQ = 64, DM = 448, HQ = 16, HKV = 8, SK = 512, DH = 256;
constexpr int RING = 65536, SLOT = 32768, LDS_BYTES = 163840;
constexpr int NTILE = 30;
constexpr float SM_C = 0.0625f * 1.4426950408889634f;
#define KV_NT 1
#define PART_AUX 16

__global__ __launch_bounds__(512, 2) void gqa_main(const char* __restrict__ X, const char* __restrict__ Kc, const char* __restrict__ Vc,
                                                  const char* __restrict__ WQ, const char* __restrict__ WO, char* __restrict__ part) {
  extern __shared__ __attribute__((aligned(16))) char smem[];
  LAS char* lds = (LAS char*)smem;
  const unsigned lds0 = (unsigned)(size_t)lds;
  const int bid = blockIdx.x, tid = threadIdx.x, l = tid & 63;
  const int w = __builtin_amdgcn_readfirstlane(tid >> 6);
  const int xcd = bid & 7, rr = bid >> 3, b = rr >> 1, role = rr & 1, h = xcd * 2 + role, kv = b * 8 + xcd;
  const int rot = (b * 7) >> 4;

  const u32x4 rsX = mk_rsrc(X + (size_t)b * (SQ * DM * 2), SQ * DM * 2);
  const u32x4 rsQ = mk_rsrc(WQ + h * 512, DM * HQ * DH * 2 - h * 512);
  const u32x4 rsK = mk_rsrc(Kc + (size_t)kv * (SK * DH * 2), SK * DH * 2);
  const u32x4 rsV = mk_rsrc(Vc + (size_t)kv * (SK * DH * 2), SK * DH * 2);
  const u32x4 rsO = mk_rsrc(WO + (size_t)h * (DH * DM * 2), DH * DM * 2);
  auto rsP = __builtin_amdgcn_make_buffer_rsrc((void*)(part + (size_t)b * (16 * 7 * 8192)), 0, 16 * 7 * 8192, 0x00020000);

  const int drow = 2 * w + (l >> 5), dpos = l & 31;
  const int voQ = drow * 8192 + ((dpos ^ ((drow & 3) << 2)) << 4);
  const int voK = drow * 512 + ((dpos ^ (drow & 15)) << 4);
  const int voV = drow * 512 + ((dpos ^ ((drow & 3) << 2)) << 4);
  const int orow = 8 * w + (l >> 3), opos = l & 7;
  const int voO = orow * 896 + ((opos ^ ((((orow >> 1) & 1) | (((orow >> 3) & 1) << 1)) << 1)) << 4);
  const int voX = orow * 896 + ((opos ^ ((orow >> 1) & 7)) << 4);

  auto dma = [&](auto sc, auto ic) {
    constexpr int s = decltype(sc)::value, i = decltype(ic)::value;
    const unsigned dst = lds0 + RING + (s % 3) * SLOT + i * 8192 + w * 1024;
    if constexpr (s < 7) {
      int j = s + rot; if (j >= 7) j -= 7;
      dma16<0>(rsQ, dst, voQ, (j * 64 + i * 16) * 8192);
    } else if constexpr (s < 15) {
      dma16<KV_NT>(rsK, dst, voK, (s - 7) * 32768 + i * 8192);
    } else if constexpr (s < 23) {
      dma16<KV_NT>(rsV, dst, voV, (s - 15) * 32768 + i * 8192);
    } else if constexpr (s < NTILE) {
      int j = s - 23 + rot; if (j >= 7) j -= 7;
      dma16<0>(rsO, dst, voO, j * 128 + i * 64 * 896);
    }
  };
#define IC(n) std::integral_constant<int, (n)>{}
  auto dma_lo = [&](auto sc) { dma(sc, IC(0)); dma(sc, IC(1)); };
  auto dma_hi = [&](auto sc) { dma(sc, IC(2)); dma(sc, IC(3)); };

  sfor<7>([&](auto pc) {
    constexpr int p0 = decltype(pc)::value;
    int p = p0 + role * 3; if (p >= 7) p -= 7;
    int j = p + rot; if (j >= 7) j -= 7;
    dma16<0>(rsX, lds0 + p * 8192 + w * 1024, voX, j * 128);
  });
  dma_lo(IC(0)); dma_hi(IC(0)); dma_lo(IC(1)); dma_hi(IC(1));

  const int hh = l >> 5, r32 = l & 31, r16 = l & 15, g4 = l >> 4;
  const int tq = (l & 15) >> 2, tp = l & 3, tblk = (l >> 4) & 1;
  const int aTr = (8 * hh + tq) * 512 + (((4 * w + 2 * tblk + (tp >> 1)) ^ (tq << 2)) << 4) + (tp & 1) * 8;
  int aSt[4];
#pragma unroll
  for (int s = 0; s < 4; ++s) aSt[s] = r32 * 128 + (((2 * s + hh) ^ ((r32 >> 1) & 7)) << 4);
  int aRow[4];
#pragma unroll
  for (int s = 0; s < 4; ++s) aRow[s] = r16 * 512 + (((4 * s + g4) ^ r16) << 4);
  const int wq2 = w >> 1, wh = w & 1;

  auto tn_read = [&](auto sc, bf16x8 (&fa)[4]) {
    constexpr int s = decltype(sc)::value;
    LAS char* base = lds + RING + (s % 3) * SLOT + aTr;
#pragma unroll
    for (int ks = 0; ks < 4; ++ks) fa[ks] = ldstr(base + ks * 8192, base + ks * 8192 + 2048);
  };
  auto tn_mma = [&](int pos, const bf16x8 (&fa)[4], f32x16 (&acc)[2]) {
#pragma unroll
    for (int ks = 0; ks < 4; ++ks) {
      bf16x8 b0 = lds128(lds + aSt[ks] + pos * 8192), b1 = lds128(lds + aSt[ks] + pos * 8192 + 4096);
      acc[0] = MFMA32(fa[ks], b0, acc[0]);
      acc[1] = MFMA32(fa[ks], b1, acc[1]);
    }
  };
  auto img_write = [&](const f32x16 (&acc)[2]) {
#pragma unroll
    for (int j = 0; j < 2; ++j)
#pragma unroll
      for (int rg = 0; rg < 4; ++rg) {
        u32x2 v = {pk2(acc[j][4 * rg], acc[j][4 * rg + 1]), pk2(acc[j][4 * rg + 2], acc[j][4 * rg + 3])};
        *(LAS u32x2*)(lds + (32 * j + r32) * 512 + (((4 * w + rg) ^ r16) << 4) + 8 * hh) = v;
      }
  };
  auto img_read = [&](bf16x8 (&f)[2][8]) {
#pragma unroll
    for (int s = 0; s < 8; ++s)
#pragma unroll
      for (int j = 0; j < 2; ++j) f[j][s] = lds128(lds + aRow[s & 3] + (s >> 2) * 256 + (32 * wh + 16 * j) * 512);
  };

  f32x16 acc[2];
#pragma unroll
  for (int i = 0; i < 16; ++i) { acc[0][i] = 0.f; acc[1][i] = 0.f; }
  {
    bf16x8 fa[2][4];
    sfor<7>([&](auto sc) {
      constexpr int s = decltype(sc)::value;
      vmwait<4>(); bar();
      dma_lo(IC(s + 2));
      tn_read(sc, fa[s & 1]);
      if constexpr (s > 0) tn_mma(s - 1, fa[(s - 1) & 1], acc);
      dma_hi(IC(s + 2));
    });
    tn_mma(6, fa[0], acc);
  }
  img_write(acc);

  f32x4 sacc[8][2];
  {
    bf16x8 qf[2][8];
    sfor<8>([&](auto tc) {
      constexpr int t = decltype(tc)::value, s = 7 + t;
      vmwait<4>(); bar();
      dma_lo(IC(s + 2));
      if constexpr (t == 0) img_read(qf);
      LAS char* base = lds + RING + (s % 3) * SLOT + wq2 * 8192;
      f32x4 c0 = {0.f, 0.f, 0.f, 0.f}, c1 = c0;
#pragma unroll
      for (int ks = 0; ks < 8; ++ks) {
        bf16x8 kf = lds128(base + aRow[ks & 3] + (ks >> 2) * 256);
        c0 = MFMA16(kf, qf[0][ks], c0);
        c1 = MFMA16(kf, qf[1][ks], c1);
      }
      sacc[t][0] = c0; sacc[t][1] = c1;
      dma_hi(IC(s + 2));
    });
  }
  float mw[2], lw[2];
#pragma unroll
  for (int j = 0; j < 2; ++j) {
    float m = sacc[0][j][0];
#pragma unroll
    for (int t = 0; t < 8; ++t)
#pragma unroll
      for (int e = 0; e < 4; ++e) m = fmaxf(m, sacc[t][j][e]);
    { auto x = __builtin_amdgcn_permlane16_swap(__float_as_uint(m), __float_as_uint(m), false, false); m = fmaxf(__uint_as_float(x[0]), __uint_as_float(x[1])); }
    { auto x = __builtin_amdgcn_permlane32_swap(__float_as_uint(m), __float_as_uint(m), false, false); m = fmaxf(__uint_as_float(x[0]), __uint_as_float(x[1])); }
    const float mc = m * SM_C;
    float sum = 0.f;
#pragma unroll
    for (int t = 0; t < 8; ++t)
#pragma unroll
      for (int e = 0; e < 4; ++e) { float p = __builtin_amdgcn_exp2f(fmaf(sacc[t][j][e], SM_C, -mc)); sacc[t][j][e] = p; sum += p; }
    { auto x = __builtin_amdgcn_permlane16_swap(__float_as_uint(sum), __float_as_uint(sum), false, false); sum = __uint_as_float(x[0]) + __uint_as_float(x[1]); }
    { auto x = __builtin_amdgcn_permlane32_swap(__float_as_uint(sum), __float_as_uint(sum), false, false); sum = __uint_as_float(x[0]) + __uint_as_float(x[1]); }
    mw[j] = mc; lw[j] = sum;
    if (g4 == 0) { f32x2 v = {mc, sum}; *(LAS f32x2*)(lds + ((32 * wh + 16 * j + r16) * 4 + wq2) * 8) = v; }
  }
  vmwait<4>(); bar();
  dma_lo(IC(17)); dma_hi(IC(17));
  u32x2 pp[8][2];
#pragma unroll
  for (int j = 0; j < 2; ++j) {
    f32x4 a = *(LAS f32x4*)(lds + (32 * wh + 16 * j + r16) * 32), c = *(LAS f32x4*)(lds + (32 * wh + 16 * j + r16) * 32 + 16);
    const float m = fmaxf(fmaxf(a[0], a[2]), fmaxf(c[0], c[2]));
    const float lsum = a[1] * __builtin_amdgcn_exp2f(a[0] - m) + a[3] * __builtin_amdgcn_exp2f(a[2] - m) + c[1] * __builtin_amdgcn_exp2f(c[0] - m) +
                       c[3] * __builtin_amdgcn_exp2f(c[2] - m);
    const float f = __builtin_amdgcn_exp2f(mw[j] - m) / lsum;
#pragma unroll
    for (int t = 0; t < 8; ++t) { pp[t][j][0] = pk2(sacc[t][j][0] * f, sacc[t][j][1] * f); pp[t][j][1] = pk2(sacc[t][j][2] * f, sacc[t][j][3] * f); }
  }
  bar();
  {
    LAS char* pb = lds + (32 * wh + r16) * 128 + (((2 * wq2 + (g4 >> 1)) ^ (r16 >> 1)) << 4) + (g4 & 1) * 8;
#pragma unroll
    for (int t = 0; t < 8; ++t)
#pragma unroll
      for (int j = 0; j < 2; ++j) *(LAS u32x2*)(pb + t * 8192 + j * 2048) = pp[t][j];
  }
  bar();

#pragma unroll
  for (int i = 0; i < 16; ++i) { acc[0][i] = 0.f; acc[1][i] = 0.f; }
  sfor<8>([&](auto tc) {
    constexpr int t = decltype(tc)::value, s = 15 + t;
    if constexpr (t > 0) { vmwait<4>(); bar(); dma_lo(IC(s + 2)); }
    bf16x8 fa[4];
    tn_read(IC(s), fa);
    tn_mma(t, fa, acc);
    if constexpr (t > 0) dma_hi(IC(s + 2));
  });
  img_write(acc);

  {
    bf16x8 of[2][8];
    bf16x8 fw[2][8];
    const int aWo = (8 * g4 + tq) * 128 + (((2 * wq2 + (tp >> 1)) ^ (((tq >> 1) | ((g4 & 1) << 1)) << 1)) << 4) + (tp & 1) * 8;
    const int voP = (2 * w + hh) * (7 * 8192) + h * 512 + r32 * 16;
    auto e_mma = [&](auto tc) {
      constexpr int t = decltype(tc)::value;
      f32x4 c0 = {0.f, 0.f, 0.f, 0.f}, c1 = c0;
#pragma unroll
      for (int ks = 0; ks < 8; ++ks) { c0 = MFMA16(fw[t & 1][ks], of[0][ks], c0); c1 = MFMA16(fw[t & 1][ks], of[1][ks], c1); }
      u32x4 v = {pk2(c0[0], c0[1]), pk2(c0[2], c0[3]), pk2(c1[0], c1[1]), pk2(c1[2], c1[3])};
      int j = t + rot; if (j >= 7) j -= 7;
      __builtin_amdgcn_raw_buffer_store_b128(v, rsP, voP, j * 8192, PART_AUX);
    };
    sfor<7>([&](auto tc) {
      constexpr int t = decltype(tc)::value, s = 23 + t;
      constexpr int n = (s + 1 < NTILE ? 4 : 0) + (t >= 2 ? 1 : 0) + (t >= 3 ? 1 : 0);
      vmwait<n>(); bar();
      dma_lo(IC(s + 2)); dma_hi(IC(s + 2));
      if constexpr (t == 0) img_read(of);
      LAS char* base = lds + RING + (s % 3) * SLOT + aWo;
#pragma unroll
      for (int ks = 0; ks < 8; ++ks) fw[t & 1][ks] = ldstr(base + ks * 4096, base + ks * 4096 + 512);
      if constexpr (t > 0) e_mma(IC(t - 1));
    });
    e_mma(IC(6));
  }
}

__global__ __launch_bounds__(448) void gqa_reduce(const char* __restrict__ part, char* __restrict__ out) {
  const int bid = blockIdx.x, tid = threadIdx.x, l = tid & 63, T = tid >> 6;
  const int b = bid >> 4, c = bid & 15;
  const GAS u32x4* p = (const GAS u32x4*)(part + ((size_t)(bid * 7 + T)) * 8192 + l * 16);
  u32x4 v[8];
#pragma unroll
  for (int u = 0; u < 8; ++u) v[u] = __builtin_nontemporal_load(p + u * 64);
  float s[8];
#pragma unroll
  for (int e = 0; e < 8; ++e) s[e] = 0.f;
#pragma unroll
  for (int u = 0; u < 8; ++u)
#pragma unroll
    for (int e = 0; e < 4; ++e) { s[2 * e] += __uint_as_float(v[u][e] << 16); s[2 * e + 1] += __uint_as_float(v[u][e] & 0xffff0000u); }
  float o[4];
#pragma unroll
  for (int e = 0; e < 4; ++e) {
    auto x = __builtin_amdgcn_permlane32_swap(__float_as_uint(s[e]), __float_as_uint(s[4 + e]), false, false);
    o[e] = __uint_as_float(x[0]) + __uint_as_float(x[1]);
  }
  const int wp = c >> 1, nq = wp >> 1, qh = wp & 1, i = l & 31, jj = l >> 5;
  const int q = 32 * qh + 16 * jj + (i & 15), n = 64 * T + 16 * nq + 4 * (2 * (c & 1) + (i >> 4));
  u32x2 r = {pk2(o[0], o[1]), pk2(o[2], o[3])};
  *(GAS u32x2*)(out + ((size_t)(b * 64 + q) * DM + n) * 2) = r;
}

extern "C" void kernel_launch(void* const* d_in, const int* in_sizes, int n_in, void* d_out, int out_size, void* d_ws, size_t ws_size,
                              hipStream_t stream) {
  (void)hipFuncSetAttribute((const void*)gqa_main, hipFuncAttributeMaxDynamicSharedMemorySize, LDS_BYTES);
  gqa_main<<<256, 512, LDS_BYTES, stream>>>((const char*)d_in[0], (const char*)d_in[1], (const char*)d_in[2], (const char*)d_in[3],
                                            (const char*)d_in[4], (char*)d_ws);
  gqa_reduce<<<256, 448, 0, stream>>>((const char*)d_ws, (char*)d_out);
}
```

```cpp
#include <hip/hip_runtime.h>
#include <hip/hip_bf16.h>
#include <utility>

#define GAS __attribute__((address_space(1)))
#define LAS __attribute__((address_space(3)))
#define DI __device__ __forceinline__
typedef short bf16x8 __attribute__((ext_vector_type(8)));
typedef short s16x4 __attribute__((ext_vector_type(4)));
typedef float f32x4 __attribute__((ext_vector_type(4)));
typedef float f32x2 __attribute__((ext_vector_type(2)));
typedef float f32x16 __attribute__((ext_vector_type(16)));
typedef unsigned u32x4 __attribute__((ext_vector_type(4)));
typedef unsigned u32x2 __attribute__((ext_vector_type(2)));
typedef __bf16 bf16x2v __attribute__((ext_vector_type(2)));

template <int... Is, class F> DI void sfor_seq(std::integer_sequence<int, Is...>, F&& f) { (f(std::integral_constant<int, Is>{}), ...); }
template <int N, class F> DI void sfor(F&& f) { sfor_seq(std::make_integer_sequence<int, N>{}, f); }
template <int N> DI void vmwait() { asm volatile("s_waitcnt vmcnt(%0)" ::"i"(N) : "memory"); }
DI void lgkm0() { asm volatile("s_waitcnt lgkmcnt(0)" ::: "memory"); }
#define SB() __builtin_amdgcn_sched_barrier(0)
DI void bar() {
  asm volatile("" ::: "memory");
  __builtin_amdgcn_s_waitcnt(0xC07F);
  __builtin_amdgcn_s_barrier();
  asm volatile("" ::: "memory");
}
DI float xmax16_32(float m) {
  { auto x = __builtin_amdgcn_permlane16_swap(__float_as_uint(m), __float_as_uint(m), false, false); m = fmaxf(__uint_as_float(x[0]), __uint_as_float(x[1])); }
  { auto x = __builtin_amdgcn_permlane32_swap(__float_as_uint(m), __float_as_uint(m), false, false); m = fmaxf(__uint_as_float(x[0]), __uint_as_float(x[1])); }
  return m;
}
DI float xsum16_32(float m) {
  { auto x = __builtin_amdgcn_permlane16_swap(__float_as_uint(m), __float_as_uint(m), false, false); m = __uint_as_float(x[0]) + __uint_as_float(x[1]); }
  { auto x = __builtin_amdgcn_permlane32_swap(__float_as_uint(m), __float_as_uint(m), false, false); m = __uint_as_float(x[0]) + __uint_as_float(x[1]); }
  return m;
}
DI unsigned pk2(float a, float b) { f32x2 v = {a, b}; return __builtin_bit_cast(unsigned, __builtin_convertvector(v, bf16x2v)); }
DI bf16x8 lds128(LAS char* p) { return *(LAS bf16x8*)p; }
DI bf16x8 ldstr(LAS char* p0, LAS char* p1) {
  s16x4 a = __builtin_amdgcn_ds_read_tr16_b64_v4i16((LAS s16x4*)p0);
  s16x4 b = __builtin_amdgcn_ds_read_tr16_b64_v4i16((LAS s16x4*)p1);
  return __builtin_shufflevector(a, b, 0, 1, 2, 3, 4, 5, 6, 7);
}
template <int NT> DI void dma16(u32x4 rs, unsigned ldsaddr, int voff, int soff) {
  if constexpr (NT) asm volatile("s_mov_b32 m0, %0\n\ts_nop 0\n\tbuffer_load_dwordx4 %1, %2, %3 offen nt lds" ::"s"(ldsaddr), "v"(voff), "s"(rs), "s"(soff) : "memory");
  else asm volatile("s_mov_b32 m0, %0\n\ts_nop 0\n\tbuffer_load_dwordx4 %1, %2, %3 offen lds" ::"s"(ldsaddr), "v"(voff), "s"(rs), "s"(soff) : "memory");
}
DI u32x4 mk_rsrc(const char* p, unsigned bytes) {
  unsigned long long a = (unsigned long long)p;
  u32x4 r = {(unsigned)a, (unsigned)(a >> 32) & 0xffffu, bytes, 0x00020000u};
  return r;
}
#define MFMA32(a, b, c) __builtin_amdgcn_mfma_f32_32x32x16_bf16((a), (b), (c), 0, 0, 0)
#define MFMA16(a, b, c) __builtin_amdgcn_mfma_f32_16x16x32_bf16((a), (b), (c), 0, 0, 0)

#define STAMP(i) do {} while (0)
constexpr int NB = 16, SQ = 64, DM = 448, HQ = 16, HKV = 8, SK = 512, DH = 256;
constexpr int RING = 65536, SLOT = 32768, LDS_BYTES = 163840;
constexpr int NTILE = 30;
constexpr float SM_C = 0.0625f * 1.4426950408889634f;
#define KV_NT 1
#define PART_AUX 16

__global__ __launch_bounds__(512, 2) void gqa_main(const char* __restrict__ X, const char* __restrict__ Kc, const char* __restrict__ Vc,
                                                  const char* __restrict__ WQ, const char* __restrict__ WO, char* __restrict__ part) {
  extern __shared__ __attribute__((aligned(16))) char smem[];
  LAS char* lds = (LAS char*)smem;
  const unsigned lds0 = (unsigned)(size_t)lds;
  STAMP(0);
  const int bid = blockIdx.x, tid = threadIdx.x, l = tid & 63;
  const int w = __builtin_amdgcn_readfirstlane(tid >> 6);
  const int xcd = bid & 7, rr = bid >> 3, b = rr >> 1, role = rr & 1, h = xcd * 2 + role, kv = b * 8 + xcd;
  const int rot = (b * 7) >> 4;

  const u32x4 rsX = mk_rsrc(X + (size_t)b * (SQ * DM * 2), SQ * DM * 2);
  const u32x4 rsQ = mk_rsrc(WQ + h * 512, DM * HQ * DH * 2 - h * 512);
  const u32x4 rsK = mk_rsrc(Kc + (size_t)kv * (SK * DH * 2), SK * DH * 2);
  const u32x4 rsV = mk_rsrc(Vc + (size_t)kv * (SK * DH * 2), SK * DH * 2);
  const u32x4 rsO = mk_rsrc(WO + (size_t)h * (DH * DM * 2), DH * DM * 2);
  auto rsP = __builtin_amdgcn_make_buffer_rsrc((void*)(part + (size_t)b * (16 * 7 * 8192)), 0, 16 * 7 * 8192, 0x00020000);

  const int drow = 2 * w + (l >> 5), dpos = l & 31;
  const int voQ = drow * 8192 + ((dpos ^ ((drow & 3) << 2)) << 4);
  const int voK = drow * 512 + ((dpos ^ (drow & 15)) << 4);
  const int voV = drow * 512 + ((dpos ^ ((drow & 3) << 2)) << 4);
  const int orow = 8 * w + (l >> 3), opos = l & 7;
  const int voO = orow * 896 + ((opos ^ ((((orow >> 1) & 1) | (((orow >> 3) & 1) << 1)) << 1)) << 4);
  const int voX = orow * 896 + ((opos ^ ((orow >> 1) & 7)) << 4);

  auto dma = [&](auto sc, auto ic) {
    constexpr int s = decltype(sc)::value, i = decltype(ic)::value;
    const unsigned dst = lds0 + RING + (s % 3) * SLOT + i * 8192 + w * 1024;
    if constexpr (s < 7) {
      int j = s + rot; if (j >= 7) j -= 7;
      dma16<0>(rsQ, dst, voQ, (j * 64 + i * 16) * 8192);
    } else if constexpr (s < 15) {
      dma16<KV_NT>(rsK, dst, voK, (s - 7) * 32768 + i * 8192);
    } else if constexpr (s < 23) {
      dma16<KV_NT>(rsV, dst, voV, (s - 15) * 32768 + i * 8192);
    } else if constexpr (s < NTILE) {
      int j = s - 23 + rot; if (j >= 7) j -= 7;
      dma16<0>(rsO, dst, voO, j * 128 + i * 64 * 896);
    }
  };
#define IC(n) std::integral_constant<int, (n)>{}
  auto dma_lo = [&](auto sc) { dma(sc, IC(0)); dma(sc, IC(1)); };
  auto dma_hi = [&](auto sc) { dma(sc, IC(2)); dma(sc, IC(3)); };

  sfor<7>([&](auto pc) {
    constexpr int p0 = decltype(pc)::value;
    int p = p0 + role * 3; if (p >= 7) p -= 7;
    int j = p + rot; if (j >= 7) j -= 7;
    dma16<0>(rsX, lds0 + p * 8192 + w * 1024, voX, j * 128);
  });
  dma_lo(IC(0)); dma_hi(IC(0)); dma_lo(IC(1)); dma_hi(IC(1));

  const int hh = l >> 5, r32 = l & 31, r16 = l & 15, g4 = l >> 4;
  const int tq = (l & 15) >> 2, tp = l & 3, tblk = (l >> 4) & 1;
  const int aTr = (8 * hh + tq) * 512 + (((4 * w + 2 * tblk + (tp >> 1)) ^ (tq << 2)) << 4) + (tp & 1) * 8;
  int aSt[4];
#pragma unroll
  for (int s = 0; s < 4; ++s) aSt[s] = r32 * 128 + (((2 * s + hh) ^ ((r32 >> 1) & 7)) << 4);
  int aRow[4];
#pragma unroll
  for (int s = 0; s < 4; ++s) aRow[s] = r16 * 512 + (((4 * s + g4) ^ r16) << 4);
  const int wq2 = w >> 1, wh = w & 1;

  struct Frag { bf16x8 a[4], b0[4], b1[4]; };
  auto tn_read = [&](int s, int pos, int ks, Frag& f) {
    LAS char* base = lds + RING + (s % 3) * SLOT + aTr;
    f.a[ks] = ldstr(base + ks * 8192, base + ks * 8192 + 2048);
    f.b0[ks] = lds128(lds + aSt[ks] + pos * 8192);
    f.b1[ks] = lds128(lds + aSt[ks] + pos * 8192 + 4096);
  };
  auto tn_mma = [&](int ks, const Frag& f, f32x16 (&acc)[2]) {
    acc[0] = MFMA32(f.a[ks], f.b0[ks], acc[0]);
    acc[1] = MFMA32(f.a[ks], f.b1[ks], acc[1]);
  };
  auto img_write = [&](const f32x16 (&acc)[2]) {
#pragma unroll
    for (int j = 0; j < 2; ++j)
#pragma unroll
      for (int rg = 0; rg < 4; ++rg) {
        u32x2 v = {pk2(acc[j][4 * rg], acc[j][4 * rg + 1]), pk2(acc[j][4 * rg + 2], acc[j][4 * rg + 3])};
        *(LAS u32x2*)(lds + (32 * j + r32) * 512 + (((4 * w + rg) ^ r16) << 4) + 8 * hh) = v;
      }
  };
  auto img_read = [&](bf16x8 (&f)[2][8]) {
#pragma unroll
    for (int s = 0; s < 8; ++s)
#pragma unroll
      for (int j = 0; j < 2; ++j) f[j][s] = lds128(lds + aRow[s & 3] + (s >> 2) * 256 + (32 * wh + 16 * j) * 512);
  };

  f32x16 acc[2];
#pragma unroll
  for (int i = 0; i < 16; ++i) { acc[0][i] = 0.f; acc[1][i] = 0.f; }
  {
    Frag fr[2];
    sfor<7>([&](auto sc) {
      constexpr int s = decltype(sc)::value;
      Frag& cur = fr[s & 1];
      const Frag& prv = fr[(s + 1) & 1];
      SB(); vmwait<4>(); bar(); STAMP(1 + s); SB();
      if constexpr (s > 0) tn_mma(0, prv, acc);
      SB(); tn_read(s, s, 0, cur); tn_read(s, s, 1, cur); SB();
      dma(IC(s + 2), IC(0)); SB();
      if constexpr (s > 0) tn_mma(1, prv, acc);
      SB(); tn_read(s, s, 2, cur); SB();
      dma(IC(s + 2), IC(1)); SB();
      if constexpr (s > 0) tn_mma(2, prv, acc);
      SB(); tn_read(s, s, 3, cur); SB();
      dma(IC(s + 2), IC(2)); SB();
      if constexpr (s > 0) tn_mma(3, prv, acc);
      SB(); dma(IC(s + 2), IC(3)); SB();
    });
#pragma unroll
    for (int ks = 0; ks < 4; ++ks) tn_mma(ks, fr[0], acc);
  }
  img_write(acc);

  f32x4 sacc[8][2];
  float mrun[2];
  {
    bf16x8 qf[2][8];
    sfor<8>([&](auto tc) {
      constexpr int t = decltype(tc)::value, s = 7 + t;
      if constexpr (t == 0) STAMP(31);
      SB(); vmwait<4>(); bar(); STAMP(1 + s); SB();
      LAS char* base = lds + RING + (s % 3) * SLOT + wq2 * 8192;
      bf16x8 kf[8];
      if constexpr (t == 0) {
#pragma unroll
        for (int ks = 0; ks < 8; ++ks) {
          kf[ks] = lds128(base + aRow[ks & 3] + (ks >> 2) * 256);
          qf[0][ks] = lds128(lds + aRow[ks & 3] + (ks >> 2) * 256 + (32 * wh) * 512);
          qf[1][ks] = lds128(lds + aRow[ks & 3] + (ks >> 2) * 256 + (32 * wh + 16) * 512);
        }
      } else {
#pragma unroll
        for (int ks = 0; ks < 8; ++ks) kf[ks] = lds128(base + aRow[ks & 3] + (ks >> 2) * 256);
      }
      SB(); dma_lo(IC(s + 2)); SB();
      if constexpr (t > 0) {
#pragma unroll
        for (int j = 0; j < 2; ++j) {
          float m = fmaxf(fmaxf(sacc[t - 1][j][0], sacc[t - 1][j][1]), fmaxf(sacc[t - 1][j][2], sacc[t - 1][j][3]));
          mrun[j] = t == 1 ? m : fmaxf(mrun[j], m);
        }
        SB();
      }
      f32x4 c0 = {0.f, 0.f, 0.f, 0.f}, c1 = c0;
#pragma unroll
      for (int ks = 0; ks < 8; ++ks) {
        c0 = MFMA16(kf[ks], qf[0][ks], c0);
        c1 = MFMA16(kf[ks], qf[1][ks], c1);
      }
      sacc[t][0] = c0; sacc[t][1] = c1;
      SB(); if constexpr (t < 7) dma_hi(IC(s + 2)); SB();
    });
  }
  STAMP(32);
  constexpr int ST_M = 7 * 8192, ST_L = 4 * 8192;
#pragma unroll
  for (int j = 0; j < 2; ++j) {
    float m = fmaxf(fmaxf(sacc[7][j][0], sacc[7][j][1]), fmaxf(sacc[7][j][2], sacc[7][j][3]));
    m = xmax16_32(fmaxf(mrun[j], m));
    if (g4 == 0) *(LAS float*)(lds + ST_M + ((32 * wh + 16 * j + r16) * 4 + wq2) * 4) = m;
  }
  SB(); dma_hi(IC(16)); SB();
  bar();
  STAMP(33);
  float mc[2], lsum[2] = {0.f, 0.f};
#pragma unroll
  for (int j = 0; j < 2; ++j) {
    f32x4 a = *(LAS f32x4*)(lds + ST_M + (32 * wh + 16 * j + r16) * 16);
    mc[j] = -SM_C * fmaxf(fmaxf(a[0], a[1]), fmaxf(a[2], a[3]));
  }
  LAS char* pb = lds + (32 * wh + r16) * 128 + (((2 * wq2 + (g4 >> 1)) ^ (r16 >> 1)) << 4) + (g4 & 1) * 8;
  auto p_write = [&](auto tc) {
    constexpr int t = decltype(tc)::value;
#pragma unroll
    for (int j = 0; j < 2; ++j) {
      float p[4];
#pragma unroll
      for (int e = 0; e < 4; ++e) p[e] = __builtin_amdgcn_exp2f(fmaf(sacc[t][j][e], SM_C, mc[j]));
      lsum[j] += (p[0] + p[1]) + (p[2] + p[3]);
      u32x2 v = {pk2(p[0], p[1]), pk2(p[2], p[3])};
      *(LAS u32x2*)(pb + t * 8192 + j * 2048) = v;
    }
  };
  p_write(IC(0));
  STAMP(34);

#pragma unroll
  for (int i = 0; i < 16; ++i) { acc[0][i] = 0.f; acc[1][i] = 0.f; }
  float linv[2];
  sfor<8>([&](auto tc) {
    constexpr int t = decltype(tc)::value, s = 15 + t;
    SB(); vmwait<4>(); bar(); STAMP(1 + s); SB();
    Frag f;
#pragma unroll
    for (int ks = 0; ks < 4; ++ks) tn_read(s, t, ks, f);
    f32x4 lp[2];
    if constexpr (t == 7) {
#pragma unroll
      for (int j = 0; j < 2; ++j) lp[j] = *(LAS f32x4*)(lds + ST_L + (32 * j + r32) * 16);
    }
    SB(); dma_lo(IC(s + 2)); SB();
#pragma unroll
    for (int ks = 0; ks < 4; ++ks) tn_mma(ks, f, acc);
    if constexpr (t < 7) p_write(IC(t + 1));
    if constexpr (t == 6) {
#pragma unroll
      for (int j = 0; j < 2; ++j) {
        float v = xsum16_32(lsum[j]);
        if (g4 == 0) *(LAS float*)(lds + ST_L + ((32 * wh + 16 * j + r16) * 4 + wq2) * 4) = v;
      }
    }
    if constexpr (t == 7) {
#pragma unroll
      for (int j = 0; j < 2; ++j) linv[j] = 1.0f / ((lp[j][0] + lp[j][1]) + (lp[j][2] + lp[j][3]));
    }
    SB(); dma_hi(IC(s + 2)); SB();
  });
#pragma unroll
  for (int i = 0; i < 16; ++i) { acc[0][i] *= linv[0]; acc[1][i] *= linv[1]; }
  img_write(acc);

  {
    bf16x8 of[2][8];
    bf16x8 fw[2][8];
    const int aWo = (8 * g4 + tq) * 128 + (((2 * wq2 + (tp >> 1)) ^ (((tq >> 1) | ((g4 & 1) << 1)) << 1)) << 4) + (tp & 1) * 8;
    const int voP = (2 * w + hh) * (7 * 8192) + h * 512 + r32 * 16;
    f32x4 c0, c1;
    auto e_mma = [&](int t, int k0, int k1) {
#pragma unroll
      for (int ks = k0; ks < k1; ++ks) {
        if (ks == 0) { c0 = f32x4{0.f, 0.f, 0.f, 0.f}; c1 = c0; }
        c0 = MFMA16(fw[t & 1][ks], of[0][ks], c0); c1 = MFMA16(fw[t & 1][ks], of[1][ks], c1);
      }
    };
    auto e_store = [&](int t) {
      u32x4 v = {pk2(c0[0], c0[1]), pk2(c0[2], c0[3]), pk2(c1[0], c1[1]), pk2(c1[2], c1[3])};
      int j = t + rot; if (j >= 7) j -= 7;
      __builtin_amdgcn_raw_buffer_store_b128(v, rsP, voP, j * 8192, PART_AUX);
    };
    sfor<7>([&](auto tc) {
      constexpr int t = decltype(tc)::value, s = 23 + t;
      constexpr int n = (s + 1 < NTILE ? 4 : 0) + (t >= 2 ? 1 : 0) + (t >= 3 ? 1 : 0);
      if constexpr (t == 0) STAMP(35);
      SB(); vmwait<n>(); bar(); STAMP(1 + s); SB();
      LAS char* base = lds + RING + (s % 3) * SLOT + aWo;
      auto rd = [&](int k0, int k1) {
#pragma unroll
        for (int ks = k0; ks < k1; ++ks) fw[t & 1][ks] = ldstr(base + ks * 4096, base + ks * 4096 + 512);
      };
      if constexpr (t == 0) {
        rd(0, 8); img_read(of); SB();
        dma_lo(IC(s + 2)); dma_hi(IC(s + 2)); SB();
      } else {
        e_mma(t - 1, 0, 2); SB(); rd(0, 3); SB(); dma(IC(s + 2), IC(0)); SB();
        e_mma(t - 1, 2, 4); SB(); rd(3, 6); SB(); dma(IC(s + 2), IC(1)); SB();
        e_mma(t - 1, 4, 6); SB(); rd(6, 8); SB(); dma(IC(s + 2), IC(2)); SB();
        e_mma(t - 1, 6, 8); SB(); dma(IC(s + 2), IC(3)); SB();
        e_store(t - 1); SB();
      }
    });
    e_mma(6, 0, 8); e_store(6);
  }
}

__global__ __launch_bounds__(448) void gqa_reduce(const char* __restrict__ part, char* __restrict__ out) {
  const int bid = blockIdx.x, tid = threadIdx.x, l = tid & 63, T = tid >> 6;
  const int b = bid >> 4, c = bid & 15;
  const GAS u32x4* p = (const GAS u32x4*)(part + ((size_t)(bid * 7 + T)) * 8192 + l * 16);
  u32x4 v[8];
#pragma unroll
  for (int u = 0; u < 8; ++u) v[u] = __builtin_nontemporal_load(p + u * 64);
  float s[8];
#pragma unroll
  for (int e = 0; e < 8; ++e) s[e] = 0.f;
#pragma unroll
  for (int u = 0; u < 8; ++u)
#pragma unroll
    for (int e = 0; e < 4; ++e) { s[2 * e] += __uint_as_float(v[u][e] << 16); s[2 * e + 1] += __uint_as_float(v[u][e] & 0xffff0000u); }
  float o[4];
#pragma unroll
  for (int e = 0; e < 4; ++e) {
    auto x = __builtin_amdgcn_permlane32_swap(__float_as_uint(s[e]), __float_as_uint(s[4 + e]), false, false);
    o[e] = __uint_as_float(x[0]) + __uint_as_float(x[1]);
  }
  const int wp = c >> 1, nq = wp >> 1, qh = wp & 1, i = l & 31, jj = l >> 5;
  const int q = 32 * qh + 16 * jj + (i & 15), n = 64 * T + 16 * nq + 4 * (2 * (c & 1) + (i >> 4));
  u32x2 r = {pk2(o[0], o[1]), pk2(o[2], o[3])};
  *(GAS u32x2*)(out + ((size_t)(b * 64 + q) * DM + n) * 2) = r;
}

extern "C" void kernel_launch(void* const* d_in, const int* in_sizes, int n_in, void* d_out, int out_size, void* d_ws, size_t ws_size,
                              hipStream_t stream) {
  (void)hipFuncSetAttribute((const void*)gqa_main, hipFuncAttributeMaxDynamicSharedMemorySize, LDS_BYTES);
  gqa_main<<<256, 512, LDS_BYTES, stream>>>((const char*)d_in[0], (const char*)d_in[1], (const char*)d_in[2], (const char*)d_in[3],
                                            (const char*)d_in[4], (char*)d_ws);
  gqa_reduce<<<256, 448, 0, stream>>>((const char*)d_ws, (char*)d_out);
}
```

```cpp
#define DIAG
#include <hip/hip_runtime.h>
#include <hip/hip_bf16.h>
#include <utility>

#define GAS __attribute__((address_space(1)))
#define LAS __attribute__((address_space(3)))
#define DI __device__ __forceinline__
typedef short bf16x8 __attribute__((ext_vector_type(8)));
typedef short s16x4 __attribute__((ext_vector_type(4)));
typedef float f32x4 __attribute__((ext_vector_type(4)));
typedef float f32x2 __attribute__((ext_vector_type(2)));
typedef float f32x16 __attribute__((ext_vector_type(16)));
typedef unsigned u32x4 __attribute__((ext_vector_type(4)));
typedef unsigned u32x2 __attribute__((ext_vector_type(2)));
typedef __bf16 bf16x2v __attribute__((ext_vector_type(2)));

template <int... Is, class F> DI void sfor_seq(std::integer_sequence<int, Is...>, F&& f) { (f(std::integral_constant<int, Is>{}), ...); }
template <int N, class F> DI void sfor(F&& f) { sfor_seq(std::make_integer_sequence<int, N>{}, f); }
template <int N> DI void vmwait() { asm volatile("s_waitcnt vmcnt(%0)" ::"i"(N) : "memory"); }
DI void lgkm0() { asm volatile("s_waitcnt lgkmcnt(0)" ::: "memory"); }
#define SB() __builtin_amdgcn_sched_barrier(0)
DI void bar() {
  asm volatile("" ::: "memory");
  __builtin_amdgcn_s_waitcnt(0xC07F);
  __builtin_amdgcn_s_barrier();
  asm volatile("" ::: "memory");
}
DI float xmax16_32(float m) {
  { auto x = __builtin_amdgcn_permlane16_swap(__float_as_uint(m), __float_as_uint(m), false, false); m = fmaxf(__uint_as_float(x[0]), __uint_as_float(x[1])); }
  { auto x = __builtin_amdgcn_permlane32_swap(__float_as_uint(m), __float_as_uint(m), false, false); m = fmaxf(__uint_as_float(x[0]), __uint_as_float(x[1])); }
  return m;
}
DI float xsum16_32(float m) {
  { auto x = __builtin_amdgcn_permlane16_swap(__float_as_uint(m), __float_as_uint(m), false, false); m = __uint_as_float(x[0]) + __uint_as_float(x[1]); }
  { auto x = __builtin_amdgcn_permlane32_swap(__float_as_uint(m), __float_as_uint(m), false, false); m = __uint_as_float(x[0]) + __uint_as_float(x[1]); }
  return m;
}
DI unsigned pk2(float a, float b) { f32x2 v = {a, b}; return __builtin_bit_cast(unsigned, __builtin_convertvector(v, bf16x2v)); }
DI bf16x8 lds128(LAS char* p) { return *(LAS bf16x8*)p; }
DI bf16x8 ldstr(LAS char* p0, LAS char* p1) {
  s16x4 a = __builtin_amdgcn_ds_read_tr16_b64_v4i16((LAS s16x4*)p0);
  s16x4 b = __builtin_amdgcn_ds_read_tr16_b64_v4i16((LAS s16x4*)p1);
  return __builtin_shufflevector(a, b, 0, 1, 2, 3, 4, 5, 6, 7);
}
template <int NT> DI void dma16(u32x4 rs, unsigned ldsaddr, int voff, int soff) {
  if constexpr (NT) asm volatile("s_mov_b32 m0, %0\n\ts_nop 0\n\tbuffer_load_dwordx4 %1, %2, %3 offen nt lds" ::"s"(ldsaddr), "v"(voff), "s"(rs), "s"(soff) : "memory");
  else asm volatile("s_mov_b32 m0, %0\n\ts_nop 0\n\tbuffer_load_dwordx4 %1, %2, %3 offen lds" ::"s"(ldsaddr), "v"(voff), "s"(rs), "s"(soff) : "memory");
}
DI u32x4 mk_rsrc(const char* p, unsigned bytes) {
  unsigned long long a = (unsigned long long)p;
  u32x4 r = {(unsigned)a, (unsigned)(a >> 32) & 0xffffu, bytes, 0x00020000u};
  return r;
}
#define MFMA32(a, b, c) __builtin_amdgcn_mfma_f32_32x32x16_bf16((a), (b), (c), 0, 0, 0)
#define MFMA16(a, b, c) __builtin_amdgcn_mfma_f32_16x16x32_bf16((a), (b), (c), 0, 0, 0)

#define NSTAMP 40
#define DIAG_OFF (128u << 20)
#define STAMP(i) do { stv[i] = (unsigned)__builtin_amdgcn_s_memrealtime(); } while (0)
constexpr int NB = 16, SQ = 64, DM = 448, HQ = 16, HKV = 8, SK = 512, DH = 256;
constexpr int RING = 65536, SLOT = 32768, LDS_BYTES = 163840;
constexpr int NTILE = 30;
constexpr float SM_C = 0.0625f * 1.4426950408889634f;
#define KV_NT 1
#define PART_AUX 16

__global__ __launch_bounds__(512, 2) void gqa_main(const char* __restrict__ X, const char* __restrict__ Kc, const char* __restrict__ Vc,
                                                  const char* __restrict__ WQ, const char* __restrict__ WO, char* __restrict__ part) {
  extern __shared__ __attribute__((aligned(16))) char smem[];
  LAS char* lds = (LAS char*)smem;
  const unsigned lds0 = (unsigned)(size_t)lds;
  unsigned stv[NSTAMP];
  for (int i = 0; i < NSTAMP; ++i) stv[i] = 0;
  STAMP(0);
  const unsigned cyc0 = (unsigned)__builtin_amdgcn_s_memtime();
  const int bid = blockIdx.x, tid = threadIdx.x, l = tid & 63;
  const int w = __builtin_amdgcn_readfirstlane(tid >> 6);
  const int xcd = bid & 7, rr = bid >> 3, b = rr >> 1, role = rr & 1, h = xcd * 2 + role, kv = b * 8 + xcd;
  const int rot = (b * 7) >> 4;

  const u32x4 rsX = mk_rsrc(X + (size_t)b * (SQ * DM * 2), SQ * DM * 2);
  const u32x4 rsQ = mk_rsrc(WQ + h * 512, DM * HQ * DH * 2 - h * 512);
  const u32x4 rsK = mk_rsrc(Kc + (size_t)kv * (SK * DH * 2), SK * DH * 2);
  const u32x4 rsV = mk_rsrc(Vc + (size_t)kv * (SK * DH * 2), SK * DH * 2);
  const u32x4 rsO = mk_rsrc(WO + (size_t)h * (DH * DM * 2), DH * DM * 2);
  auto rsP = __builtin_amdgcn_make_buffer_rsrc((void*)(part + (size_t)b * (16 * 7 * 8192)), 0, 16 * 7 * 8192, 0x00020000);

  const int drow = 2 * w + (l >> 5), dpos = l & 31;
  const int voQ = drow * 8192 + ((dpos ^ ((drow & 3) << 2)) << 4);
  const int voK = drow * 512 + ((dpos ^ (drow & 15)) << 4);
  const int voV = drow * 512 + ((dpos ^ ((drow & 3) << 2)) << 4);
  const int orow = 8 * w + (l >> 3), opos = l & 7;
  const int voO = orow * 896 + ((opos ^ ((((orow >> 1) & 1) | (((orow >> 3) & 1) << 1)) << 1)) << 4);
  const int voX = orow * 896 + ((opos ^ ((orow >> 1) & 7)) << 4);

  auto dma = [&](auto sc, auto ic) {
    constexpr int s = decltype(sc)::value, i = decltype(ic)::value;
    const unsigned dst = lds0 + RING + (s % 3) * SLOT + i * 8192 + w * 1024;
    if constexpr (s < 7) {
      int j = s + rot; if (j >= 7) j -= 7;
      dma16<0>(rsQ, dst, voQ, (j * 64 + i * 16) * 8192);
    } else if constexpr (s < 15) {
      dma16<KV_NT>(rsK, dst, voK, (s - 7) * 32768 + i * 8192);
    } else if constexpr (s < 23) {
      dma16<KV_NT>(rsV, dst, voV, (s - 15) * 32768 + i * 8192);
    } else if constexpr (s < NTILE) {
      int j = s - 23 + rot; if (j >= 7) j -= 7;
      dma16<0>(rsO, dst, voO, j * 128 + i * 64 * 896);
    }
  };
#define IC(n) std::integral_constant<int, (n)>{}
  auto dma_lo = [&](auto sc) { dma(sc, IC(0)); dma(sc, IC(1)); };
  auto dma_hi = [&](auto sc) { dma(sc, IC(2)); dma(sc, IC(3)); };

  sfor<7>([&](auto pc) {
    constexpr int p0 = decltype(pc)::value;
    int p = p0 + role * 3; if (p >= 7) p -= 7;
    int j = p + rot; if (j >= 7) j -= 7;
    dma16<0>(rsX, lds0 + p * 8192 + w * 1024, voX, j * 128);
  });
  dma_lo(IC(0)); dma_hi(IC(0)); dma_lo(IC(1)); dma_hi(IC(1));

  const int hh = l >> 5, r32 = l & 31, r16 = l & 15, g4 = l >> 4;
  const int tq = (l & 15) >> 2, tp = l & 3, tblk = (l >> 4) & 1;
  const int aTr = (8 * hh + tq) * 512 + (((4 * w + 2 * tblk + (tp >> 1)) ^ (tq << 2)) << 4) + (tp & 1) * 8;
  int aSt[4];
#pragma unroll
  for (int s = 0; s < 4; ++s) aSt[s] = r32 * 128 + (((2 * s + hh) ^ ((r32 >> 1) & 7)) << 4);
  int aRow[4];
#pragma unroll
  for (int s = 0; s < 4; ++s) aRow[s] = r16 * 512 + (((4 * s + g4) ^ r16) << 4);
  const int wq2 = w >> 1, wh = w & 1;

  struct Frag { bf16x8 a[4], b0[4], b1[4]; };
  auto tn_read = [&](int s, int pos, int ks, Frag& f) {
    LAS char* base = lds + RING + (s % 3) * SLOT + aTr;
    f.a[ks] = ldstr(base + ks * 8192, base + ks * 8192 + 2048);
    f.b0[ks] = lds128(lds + aSt[ks] + pos * 8192);
    f.b1[ks] = lds128(lds + aSt[ks] + pos * 8192 + 4096);
  };
  auto tn_mma = [&](int ks, const Frag& f, f32x16 (&acc)[2]) {
    acc[0] = MFMA32(f.a[ks], f.b0[ks], acc[0]);
    acc[1] = MFMA32(f.a[ks], f.b1[ks], acc[1]);
  };
  auto img_write = [&](const f32x16 (&acc)[2]) {
#pragma unroll
    for (int j = 0; j < 2; ++j)
#pragma unroll
      for (int rg = 0; rg < 4; ++rg) {
        u32x2 v = {pk2(acc[j][4 * rg], acc[j][4 * rg + 1]), pk2(acc[j][4 * rg + 2], acc[j][4 * rg + 3])};
        *(LAS u32x2*)(lds + (32 * j + r32) * 512 + (((4 * w + rg) ^ r16) << 4) + 8 * hh) = v;
      }
  };
  auto img_read = [&](bf16x8 (&f)[2][8]) {
#pragma unroll
    for (int s = 0; s < 8; ++s)
#pragma unroll
      for (int j = 0; j < 2; ++j) f[j][s] = lds128(lds + aRow[s & 3] + (s >> 2) * 256 + (32 * wh + 16 * j) * 512);
  };

  f32x16 acc[2];
#pragma unroll
  for (int i = 0; i < 16; ++i) { acc[0][i] = 0.f; acc[1][i] = 0.f; }
  {
    Frag fr[2];
    sfor<7>([&](auto sc) {
      constexpr int s = decltype(sc)::value;
      Frag& cur = fr[s & 1];
      const Frag& prv = fr[(s + 1) & 1];
      SB(); vmwait<4>(); bar(); STAMP(1 + s); SB();
      if constexpr (s > 0) tn_mma(0, prv, acc);
      SB(); tn_read(s, s, 0, cur); tn_read(s, s, 1, cur); SB();
      dma(IC(s + 2), IC(0)); SB();
      if constexpr (s > 0) tn_mma(1, prv, acc);
      SB(); tn_read(s, s, 2, cur); SB();
      dma(IC(s + 2), IC(1)); SB();
      if constexpr (s > 0) tn_mma(2, prv, acc);
      SB(); tn_read(s, s, 3, cur); SB();
      dma(IC(s + 2), IC(2)); SB();
      if constexpr (s > 0) tn_mma(3, prv, acc);
      SB(); dma(IC(s + 2), IC(3)); SB();
    });
#pragma unroll
    for (int ks = 0; ks < 4; ++ks) tn_mma(ks, fr[0], acc);
  }
  img_write(acc);

  f32x4 sacc[8][2];
  float mrun[2];
  {
    bf16x8 qf[2][8];
    sfor<8>([&](auto tc) {
      constexpr int t = decltype(tc)::value, s = 7 + t;
      if constexpr (t == 0) STAMP(31);
      SB(); vmwait<4>(); bar(); STAMP(1 + s); SB();
      LAS char* base = lds + RING + (s % 3) * SLOT + wq2 * 8192;
      bf16x8 kf[8];
      if constexpr (t == 0) {
#pragma unroll
        for (int ks = 0; ks < 8; ++ks) {
          kf[ks] = lds128(base + aRow[ks & 3] + (ks >> 2) * 256);
          qf[0][ks] = lds128(lds + aRow[ks & 3] + (ks >> 2) * 256 + (32 * wh) * 512);
          qf[1][ks] = lds128(lds + aRow[ks & 3] + (ks >> 2) * 256 + (32 * wh + 16) * 512);
        }
      } else {
#pragma unroll
        for (int ks = 0; ks < 8; ++ks) kf[ks] = lds128(base + aRow[ks & 3] + (ks >> 2) * 256);
      }
      SB(); dma_lo(IC(s + 2)); SB();
      if constexpr (t > 0) {
#pragma unroll
        for (int j = 0; j < 2; ++j) {
          float m = fmaxf(fmaxf(sacc[t - 1][j][0], sacc[t - 1][j][1]), fmaxf(sacc[t - 1][j][2], sacc[t - 1][j][3]));
          mrun[j] = t == 1 ? m : fmaxf(mrun[j], m);
        }
        SB();
      }
      f32x4 c0 = {0.f, 0.f, 0.f, 0.f}, c1 = c0;
#pragma unroll
      for (int ks = 0; ks < 8; ++ks) {
        c0 = MFMA16(kf[ks], qf[0][ks], c0);
        c1 = MFMA16(kf[ks], qf[1][ks], c1);
      }
      sacc[t][0] = c0; sacc[t][1] = c1;
      SB(); if constexpr (t < 7) dma_hi(IC(s + 2)); SB();
    });
  }
  STAMP(32);
  constexpr int ST_M = 7 * 8192, ST_L = 4 * 8192;
#pragma unroll
  for (int j = 0; j < 2; ++j) {
    float m = fmaxf(fmaxf(sacc[7][j][0], sacc[7][j][1]), fmaxf(sacc[7][j][2], sacc[7][j][3]));
    m = xmax16_32(fmaxf(mrun[j], m));
    if (g4 == 0) *(LAS float*)(lds + ST_M + ((32 * wh + 16 * j + r16) * 4 + wq2) * 4) = m;
  }
  SB(); dma_hi(IC(16)); SB();
  bar();
  STAMP(33);
  float mc[2], lsum[2] = {0.f, 0.f};
#pragma unroll
  for (int j = 0; j < 2; ++j) {
    f32x4 a = *(LAS f32x4*)(lds + ST_M + (32 * wh + 16 * j + r16) * 16);
    mc[j] = -SM_C * fmaxf(fmaxf(a[0], a[1]), fmaxf(a[2], a[3]));
  }
  LAS char* pb = lds + (32 * wh + r16) * 128 + (((2 * wq2 + (g4 >> 1)) ^ (r16 >> 1)) << 4) + (g4 & 1) * 8;
  auto p_write = [&](auto tc) {
    constexpr int t = decltype(tc)::value;
#pragma unroll
    for (int j = 0; j < 2; ++j) {
      float p[4];
#pragma unroll
      for (int e = 0; e < 4; ++e) p[e] = __builtin_amdgcn_exp2f(fmaf(sacc[t][j][e], SM_C, mc[j]));
      lsum[j] += (p[0] + p[1]) + (p[2] + p[3]);
      u32x2 v = {pk2(p[0], p[1]), pk2(p[2], p[3])};
      *(LAS u32x2*)(pb + t * 8192 + j * 2048) = v;
    }
  };
  p_write(IC(0));
  STAMP(34);

#pragma unroll
  for (int i = 0; i < 16; ++i) { acc[0][i] = 0.f; acc[1][i] = 0.f; }
  float linv[2];
  sfor<8>([&](auto tc) {
    constexpr int t = decltype(tc)::value, s = 15 + t;
    SB(); vmwait<4>(); bar(); STAMP(1 + s); SB();
    Frag f;
#pragma unroll
    for (int ks = 0; ks < 4; ++ks) tn_read(s, t, ks, f);
    f32x4 lp[2];
    if constexpr (t == 7) {
#pragma unroll
      for (int j = 0; j < 2; ++j) lp[j] = *(LAS f32x4*)(lds + ST_L + (32 * j + r32) * 16);
    }
    SB(); dma_lo(IC(s + 2)); SB();
#pragma unroll
    for (int ks = 0; ks < 4; ++ks) tn_mma(ks, f, acc);
    if constexpr (t < 7) p_write(IC(t + 1));
    if constexpr (t == 6) {
#pragma unroll
      for (int j = 0; j < 2; ++j) {
        float v = xsum16_32(lsum[j]);
        if (g4 == 0) *(LAS float*)(lds + ST_L + ((32 * wh + 16 * j + r16) * 4 + wq2) * 4) = v;
      }
    }
    if constexpr (t == 7) {
#pragma unroll
      for (int j = 0; j < 2; ++j) linv[j] = 1.0f / ((lp[j][0] + lp[j][1]) + (lp[j][2] + lp[j][3]));
    }
    SB(); dma_hi(IC(s + 2)); SB();
  });
#pragma unroll
  for (int i = 0; i < 16; ++i) { acc[0][i] *= linv[0]; acc[1][i] *= linv[1]; }
  img_write(acc);

  {
    bf16x8 of[2][8];
    bf16x8 fw[2][8];
    const int aWo = (8 * g4 + tq) * 128 + (((2 * wq2 + (tp >> 1)) ^ (((tq >> 1) | ((g4 & 1) << 1)) << 1)) << 4) + (tp & 1) * 8;
    const int voP = (2 * w + hh) * (7 * 8192) + h * 512 + r32 * 16;
    f32x4 c0, c1;
    auto e_mma = [&](int t, int k0, int k1) {
#pragma unroll
      for (int ks = k0; ks < k1; ++ks) {
        if (ks == 0) { c0 = f32x4{0.f, 0.f, 0.f, 0.f}; c1 = c0; }
        c0 = MFMA16(fw[t & 1][ks], of[0][ks], c0); c1 = MFMA16(fw[t & 1][ks], of[1][ks], c1);
      }
    };
    auto e_store = [&](int t) {
      u32x4 v = {pk2(c0[0], c0[1]), pk2(c0[2], c0[3]), pk2(c1[0], c1[1]), pk2(c1[2], c1[3])};
      int j = t + rot; if (j >= 7) j -= 7;
      __builtin_amdgcn_raw_buffer_store_b128(v, rsP, voP, j * 8192, PART_AUX);
    };
    sfor<7>([&](auto tc) {
      constexpr int t = decltype(tc)::value, s = 23 + t;
      constexpr int n = (s + 1 < NTILE ? 4 : 0) + (t >= 2 ? 1 : 0) + (t >= 3 ? 1 : 0);
      if constexpr (t == 0) STAMP(35);
      SB(); vmwait<n>(); bar(); STAMP(1 + s); SB();
      LAS char* base = lds + RING + (s % 3) * SLOT + aWo;
      auto rd = [&](int k0, int k1) {
#pragma unroll
        for (int ks = k0; ks < k1; ++ks) fw[t & 1][ks] = ldstr(base + ks * 4096, base + ks * 4096 + 512);
      };
      if constexpr (t == 0) {
        rd(0, 8); img_read(of); SB();
        dma_lo(IC(s + 2)); dma_hi(IC(s + 2)); SB();
      } else {
        e_mma(t - 1, 0, 2); SB(); rd(0, 3); SB(); dma(IC(s + 2), IC(0)); SB();
        e_mma(t - 1, 2, 4); SB(); rd(3, 6); SB(); dma(IC(s + 2), IC(1)); SB();
        e_mma(t - 1, 4, 6); SB(); rd(6, 8); SB(); dma(IC(s + 2), IC(2)); SB();
        e_mma(t - 1, 6, 8); SB(); dma(IC(s + 2), IC(3)); SB();
        e_store(t - 1); SB();
      }
    });
    e_mma(6, 0, 8); e_store(6);
  }
  STAMP(36);
  vmwait<0>();
  STAMP(37);
  stv[39] = (unsigned)__builtin_amdgcn_s_memtime() - cyc0;
  if (tid == 0) { unsigned* sp = (unsigned*)(part + DIAG_OFF) + bid * 64; sfor<NSTAMP>([&](auto ic) { sp[decltype(ic)::value] = stv[decltype(ic)::value]; }); }
}

__global__ __launch_bounds__(448) void gqa_reduce(const char* __restrict__ part, char* __restrict__ out) {
  const int bid = blockIdx.x, tid = threadIdx.x, l = tid & 63, T = tid >> 6;
  const int b = bid >> 4, c = bid & 15;
  if (tid == 0) ((unsigned*)(part + DIAG_OFF))[bid * 64 + 40] = (unsigned)__builtin_amdgcn_s_memrealtime();
  const GAS u32x4* p = (const GAS u32x4*)(part + ((size_t)(bid * 7 + T)) * 8192 + l * 16);
  u32x4 v[8];
#pragma unroll
  for (int u = 0; u < 8; ++u) v[u] = __builtin_nontemporal_load(p + u * 64);
  float s[8];
#pragma unroll
  for (int e = 0; e < 8; ++e) s[e] = 0.f;
#pragma unroll
  for (int u = 0; u < 8; ++u)
#pragma unroll
    for (int e = 0; e < 4; ++e) { s[2 * e] += __uint_as_float(v[u][e] << 16); s[2 * e + 1] += __uint_as_float(v[u][e] & 0xffff0000u); }
  float o[4];
#pragma unroll
  for (int e = 0; e < 4; ++e) {
    auto x = __builtin_amdgcn_permlane32_swap(__float_as_uint(s[e]), __float_as_uint(s[4 + e]), false, false);
    o[e] = __uint_as_float(x[0]) + __uint_as_float(x[1]);
  }
  const int wp = c >> 1, nq = wp >> 1, qh = wp & 1, i = l & 31, jj = l >> 5;
  const int q = 32 * qh + 16 * jj + (i & 15), n = 64 * T + 16 * nq + 4 * (2 * (c & 1) + (i >> 4));
  u32x2 r = {pk2(o[0], o[1]), pk2(o[2], o[3])};
  *(GAS u32x2*)(out + ((size_t)(b * 64 + q) * DM + n) * 2) = r;
  vmwait<0>();
  if (tid == 0) ((unsigned*)(part + DIAG_OFF))[bid * 64 + 41] = (unsigned)__builtin_amdgcn_s_memrealtime();
}

__global__ void probe_delay(const unsigned* st) {
  __shared__ unsigned sh[256 * 3];
  const int t = threadIdx.x;
  sh[t] = st[t * 64]; sh[256 + t] = st[t * 64 + 40]; sh[512 + t] = st[t * 64 + 0];
  __syncthreads();
  if (t == 0) {
    unsigned mn = sh[0];
    for (int i = 0; i < 256; ++i) if ((int)(sh[i] - mn) < 0) mn = sh[i];
    int va = 0, vb = 0;
    for (int i = 0; i < 256; ++i) { int a = (int)(sh[256 + i] - mn), b = (int)(sh[512 + i] - mn);
      if ('a' == 'a') va += a; else va = a > va ? a : va;
      if ('m' == 'a') vb += b; else vb = b > vb ? b : vb; }
    if ('a' == 'a') va /= 256;
    if ('m' == 'a') vb /= 256;
    va = (va + 5) / 10; vb = (vb + 5) / 10;
    va = va < 0 ? 0 : (va > 399 ? 399 : va); vb = vb < 0 ? 0 : (vb > 399 ? 399 : vb);
    unsigned long long d = (unsigned long long)va * 500 + (unsigned long long)(vb / 2) * 200000 / 1;
    unsigned long long t0 = __builtin_amdgcn_s_memrealtime();
    while (__builtin_amdgcn_s_memrealtime() - t0 < d) __builtin_amdgcn_s_sleep(2);
  }
}
extern "C" void kernel_launch(void* const* d_in, const int* in_sizes, int n_in, void* d_out, int out_size, void* d_ws, size_t ws_size,
                              hipStream_t stream) {
  (void)hipFuncSetAttribute((const void*)gqa_main, hipFuncAttributeMaxDynamicSharedMemorySize, LDS_BYTES);
  gqa_main<<<256, 512, LDS_BYTES, stream>>>((const char*)d_in[0], (const char*)d_in[1], (const char*)d_in[2], (const char*)d_in[3],
                                            (const char*)d_in[4], (char*)d_ws);
  gqa_reduce<<<256, 448, 0, stream>>>((const char*)d_ws, (char*)d_out);
  probe_delay<<<1, 256, 0, stream>>>((const unsigned*)((char*)d_ws + DIAG_OFF));
}
```

```cpp
#include <hip/hip_runtime.h>
#include <hip/hip_bf16.h>
#include <utility>

#define GAS __attribute__((address_space(1)))
#define LAS __attribute__((address_space(3)))
#define DI __device__ __forceinline__
typedef short bf16x8 __attribute__((ext_vector_type(8)));
typedef short s16x4 __attribute__((ext_vector_type(4)));
typedef float f32x4 __attribute__((ext_vector_type(4)));
typedef float f32x2 __attribute__((ext_vector_type(2)));
typedef float f32x16 __attribute__((ext_vector_type(16)));
typedef unsigned u32x4 __attribute__((ext_vector_type(4)));
typedef unsigned u32x2 __attribute__((ext_vector_type(2)));
typedef __bf16 bf16x2v __attribute__((ext_vector_type(2)));

template <int... Is, class F> DI void sfor_seq(std::integer_sequence<int, Is...>, F&& f) { (f(std::integral_constant<int, Is>{}), ...); }
template <int N, class F> DI void sfor(F&& f) { sfor_seq(std::make_integer_sequence<int, N>{}, f); }
template <int N> DI void vmwait() { asm volatile("s_waitcnt vmcnt(%0)" ::"i"(N) : "memory"); }
DI void lgkm0() { asm volatile("s_waitcnt lgkmcnt(0)" ::: "memory"); }
#define SB() __builtin_amdgcn_sched_barrier(0)
DI void bar() {
  asm volatile("" ::: "memory");
  __builtin_amdgcn_s_waitcnt(0xC07F);
  __builtin_amdgcn_s_barrier();
  asm volatile("" ::: "memory");
}
DI float xmax16_32(float m) {
  { auto x = __builtin_amdgcn_permlane16_swap(__float_as_uint(m), __float_as_uint(m), false, false); m = fmaxf(__uint_as_float(x[0]), __uint_as_float(x[1])); }
  { auto x = __builtin_amdgcn_permlane32_swap(__float_as_uint(m), __float_as_uint(m), false, false); m = fmaxf(__uint_as_float(x[0]), __uint_as_float(x[1])); }
  return m;
}
DI float xsum16_32(float m) {
  { auto x = __builtin_amdgcn_permlane16_swap(__float_as_uint(m), __float_as_uint(m), false, false); m = __uint_as_float(x[0]) + __uint_as_float(x[1]); }
  { auto x = __builtin_amdgcn_permlane32_swap(__float_as_uint(m), __float_as_uint(m), false, false); m = __uint_as_float(x[0]) + __uint_as_float(x[1]); }
  return m;
}
DI unsigned pk2(float a, float b) { f32x2 v = {a, b}; return __builtin_bit_cast(unsigned, __builtin_convertvector(v, bf16x2v)); }
DI bf16x8 lds128(LAS char* p) { return *(LAS bf16x8*)p; }
DI bf16x8 ldstr(LAS char* p0, LAS char* p1) {
  s16x4 a = __builtin_amdgcn_ds_read_tr16_b64_v4i16((LAS s16x4*)p0);
  s16x4 b = __builtin_amdgcn_ds_read_tr16_b64_v4i16((LAS s16x4*)p1);
  return __builtin_shufflevector(a, b, 0, 1, 2, 3, 4, 5, 6, 7);
}
template <int NT> DI void dma16(u32x4 rs, unsigned ldsaddr, int voff, int soff) {
  if constexpr (NT) asm volatile("s_mov_b32 m0, %0\n\ts_nop 0\n\tbuffer_load_dwordx4 %1, %2, %3 offen nt lds" ::"s"(ldsaddr), "v"(voff), "s"(rs), "s"(soff) : "memory");
  else asm volatile("s_mov_b32 m0, %0\n\ts_nop 0\n\tbuffer_load_dwordx4 %1, %2, %3 offen lds" ::"s"(ldsaddr), "v"(voff), "s"(rs), "s"(soff) : "memory");
}
DI u32x4 mk_rsrc(const char* p, unsigned bytes) {
  unsigned long long a = (unsigned long long)p;
  u32x4 r = {(unsigned)a, (unsigned)(a >> 32) & 0xffffu, bytes, 0x00020000u};
  return r;
}
#define MFMA32(a, b, c) __builtin_amdgcn_mfma_f32_32x32x16_bf16((a), (b), (c), 0, 0, 0)
#define MFMA16(a, b, c) __builtin_amdgcn_mfma_f32_16x16x32_bf16((a), (b), (c), 0, 0, 0)

#define STAMP(i) do {} while (0)
constexpr int NB = 16, SQ = 64, DM = 448, HQ = 16, HKV = 8, SK = 512, DH = 256;
constexpr int RING = 65536, SLOT = 32768, LDS_BYTES = 163840;
constexpr int NTILE = 30;
constexpr size_t FLAG_OFF = 16u << 20, EPOCH_OFF = FLAG_OFF + (1u << 20);
typedef unsigned long long u64;
#define RLX_AGENT __ATOMIC_RELAXED, __HIP_MEMORY_SCOPE_AGENT
constexpr float SM_C = 0.0625f * 1.4426950408889634f;
#define KV_NT 1
#define PART_AUX 16

__global__ __launch_bounds__(512, 2) void gqa_main(const char* __restrict__ X, const char* __restrict__ Kc, const char* __restrict__ Vc,
                                                  const char* __restrict__ WQ, const char* __restrict__ WO, char* __restrict__ part, char* __restrict__ out) {
  extern __shared__ __attribute__((aligned(16))) char smem[];
  LAS char* lds = (LAS char*)smem;
  const unsigned lds0 = (unsigned)(size_t)lds;
  STAMP(0);
  const int bid = blockIdx.x, tid = threadIdx.x, l = tid & 63;
  const int w = __builtin_amdgcn_readfirstlane(tid >> 6);
  const int xcd = bid & 7, rr = bid >> 3, b = rr >> 1, role = rr & 1, h = xcd * 2 + role, kv = b * 8 + xcd;
  const int rot = (b * 7) >> 4;

  const u32x4 rsX = mk_rsrc(X + (size_t)b * (SQ * DM * 2), SQ * DM * 2);
  const u32x4 rsQ = mk_rsrc(WQ + h * 512, DM * HQ * DH * 2 - h * 512);
  const u32x4 rsK = mk_rsrc(Kc + (size_t)kv * (SK * DH * 2), SK * DH * 2);
  const u32x4 rsV = mk_rsrc(Vc + (size_t)kv * (SK * DH * 2), SK * DH * 2);
  const u32x4 rsO = mk_rsrc(WO + (size_t)h * (DH * DM * 2), DH * DM * 2);
  auto rsP = __builtin_amdgcn_make_buffer_rsrc((void*)(part + (size_t)b * (16 * 7 * 8192)), 0, 16 * 7 * 8192, 0x00020000);

  const int drow = 2 * w + (l >> 5), dpos = l & 31;
  const int voQ = drow * 8192 + ((dpos ^ ((drow & 3) << 2)) << 4);
  const int voK = drow * 512 + ((dpos ^ (drow & 15)) << 4);
  const int voV = drow * 512 + ((dpos ^ ((drow & 3) << 2)) << 4);
  const int orow = 8 * w + (l >> 3), opos = l & 7;
  const int voO = orow * 896 + ((opos ^ ((((orow >> 1) & 1) | (((orow >> 3) & 1) << 1)) << 1)) << 4);
  const int voX = orow * 896 + ((opos ^ ((orow >> 1) & 7)) << 4);

  auto dma = [&](auto sc, auto ic) {
    constexpr int s = decltype(sc)::value, i = decltype(ic)::value;
    const unsigned dst = lds0 + RING + (s % 3) * SLOT + i * 8192 + w * 1024;
    if constexpr (s < 7) {
      int j = s + rot; if (j >= 7) j -= 7;
      dma16<0>(rsQ, dst, voQ, (j * 64 + i * 16) * 8192);
    } else if constexpr (s < 15) {
      dma16<KV_NT>(rsK, dst, voK, (s - 7) * 32768 + i * 8192);
    } else if constexpr (s < 23) {
      dma16<KV_NT>(rsV, dst, voV, (s - 15) * 32768 + i * 8192);
    } else if constexpr (s < NTILE) {
      int j = s - 23 + rot; if (j >= 7) j -= 7;
      dma16<0>(rsO, dst, voO, j * 128 + i * 64 * 896);
    }
  };
#define IC(n) std::integral_constant<int, (n)>{}
  auto dma_lo = [&](auto sc) { dma(sc, IC(0)); dma(sc, IC(1)); };
  auto dma_hi = [&](auto sc) { dma(sc, IC(2)); dma(sc, IC(3)); };

  sfor<7>([&](auto pc) {
    constexpr int p0 = decltype(pc)::value;
    int p = p0 + role * 3; if (p >= 7) p -= 7;
    int j = p + rot; if (j >= 7) j -= 7;
    dma16<0>(rsX, lds0 + p * 8192 + w * 1024, voX, j * 128);
  });
  dma_lo(IC(0)); dma_hi(IC(0)); dma_lo(IC(1)); dma_hi(IC(1));

  u64 tag;
  {
    const char* ep = part + EPOCH_OFF + b * 128;
    asm volatile("s_load_dwordx2 %0, %1, 0x0\n\ts_waitcnt lgkmcnt(0)" : "=s"(tag) : "s"(ep) : "memory");
    tag = tag * 6364136223846793005ull + 1442695040888963407ull;
  }
  const int hh = l >> 5, r32 = l & 31, r16 = l & 15, g4 = l >> 4;
  const int tq = (l & 15) >> 2, tp = l & 3, tblk = (l >> 4) & 1;
  const int aTr = (8 * hh + tq) * 512 + (((4 * w + 2 * tblk + (tp >> 1)) ^ (tq << 2)) << 4) + (tp & 1) * 8;
  int aSt[4];
#pragma unroll
  for (int s = 0; s < 4; ++s) aSt[s] = r32 * 128 + (((2 * s + hh) ^ ((r32 >> 1) & 7)) << 4);
  int aRow[4];
#pragma unroll
  for (int s = 0; s < 4; ++s) aRow[s] = r16 * 512 + (((4 * s + g4) ^ r16) << 4);
  const int wq2 = w >> 1, wh = w & 1;

  struct Frag { bf16x8 a[4], b0[4], b1[4]; };
  auto tn_read = [&](int s, int pos, int ks, Frag& f) {
    LAS char* base = lds + RING + (s % 3) * SLOT + aTr;
    f.a[ks] = ldstr(base + ks * 8192, base + ks * 8192 + 2048);
    f.b0[ks] = lds128(lds + aSt[ks] + pos * 8192);
    f.b1[ks] = lds128(lds + aSt[ks] + pos * 8192 + 4096);
  };
  auto tn_mma = [&](int ks, const Frag& f, f32x16 (&acc)[2]) {
    acc[0] = MFMA32(f.a[ks], f.b0[ks], acc[0]);
    acc[1] = MFMA32(f.a[ks], f.b1[ks], acc[1]);
  };
  auto img_write = [&](const f32x16 (&acc)[2]) {
#pragma unroll
    for (int j = 0; j < 2; ++j)
#pragma unroll
      for (int rg = 0; rg < 4; ++rg) {
        u32x2 v = {pk2(acc[j][4 * rg], acc[j][4 * rg + 1]), pk2(acc[j][4 * rg + 2], acc[j][4 * rg + 3])};
        *(LAS u32x2*)(lds + (32 * j + r32) * 512 + (((4 * w + rg) ^ r16) << 4) + 8 * hh) = v;
      }
  };
  auto img_read = [&](bf16x8 (&f)[2][8]) {
#pragma unroll
    for (int s = 0; s < 8; ++s)
#pragma unroll
      for (int j = 0; j < 2; ++j) f[j][s] = lds128(lds + aRow[s & 3] + (s >> 2) * 256 + (32 * wh + 16 * j) * 512);
  };

  f32x16 acc[2];
#pragma unroll
  for (int i = 0; i < 16; ++i) { acc[0][i] = 0.f; acc[1][i] = 0.f; }
  {
    Frag fr[2];
    sfor<7>([&](auto sc) {
      constexpr int s = decltype(sc)::value;
      Frag& cur = fr[s & 1];
      const Frag& prv = fr[(s + 1) & 1];
      SB(); vmwait<4>(); bar(); STAMP(1 + s); SB();
      if constexpr (s > 0) tn_mma(0, prv, acc);
      SB(); tn_read(s, s, 0, cur); tn_read(s, s, 1, cur); SB();
      dma(IC(s + 2), IC(0)); SB();
      if constexpr (s > 0) tn_mma(1, prv, acc);
      SB(); tn_read(s, s, 2, cur); SB();
      dma(IC(s + 2), IC(1)); SB();
      if constexpr (s > 0) tn_mma(2, prv, acc);
      SB(); tn_read(s, s, 3, cur); SB();
      dma(IC(s + 2), IC(2)); SB();
      if constexpr (s > 0) tn_mma(3, prv, acc);
      SB(); dma(IC(s + 2), IC(3)); SB();
    });
#pragma unroll
    for (int ks = 0; ks < 4; ++ks) tn_mma(ks, fr[0], acc);
  }
  img_write(acc);

  f32x4 sacc[8][2];
  float mrun[2];
  {
    bf16x8 qf[2][8];
    sfor<8>([&](auto tc) {
      constexpr int t = decltype(tc)::value, s = 7 + t;
      if constexpr (t == 0) STAMP(31);
      SB(); vmwait<4>(); bar(); STAMP(1 + s); SB();
      LAS char* base = lds + RING + (s % 3) * SLOT + wq2 * 8192;
      bf16x8 kf[8];
      if constexpr (t == 0) {
#pragma unroll
        for (int ks = 0; ks < 8; ++ks) {
          kf[ks] = lds128(base + aRow[ks & 3] + (ks >> 2) * 256);
          qf[0][ks] = lds128(lds + aRow[ks & 3] + (ks >> 2) * 256 + (32 * wh) * 512);
          qf[1][ks] = lds128(lds + aRow[ks & 3] + (ks >> 2) * 256 + (32 * wh + 16) * 512);
        }
      } else {
#pragma unroll
        for (int ks = 0; ks < 8; ++ks) kf[ks] = lds128(base + aRow[ks & 3] + (ks >> 2) * 256);
      }
      SB(); dma_lo(IC(s + 2)); SB();
      if constexpr (t > 0) {
#pragma unroll
        for (int j = 0; j < 2; ++j) {
          float m = fmaxf(fmaxf(sacc[t - 1][j][0], sacc[t - 1][j][1]), fmaxf(sacc[t - 1][j][2], sacc[t - 1][j][3]));
          mrun[j] = t == 1 ? m : fmaxf(mrun[j], m);
        }
        SB();
      }
      f32x4 c0 = {0.f, 0.f, 0.f, 0.f}, c1 = c0;
#pragma unroll
      for (int ks = 0; ks < 8; ++ks) {
        c0 = MFMA16(kf[ks], qf[0][ks], c0);
        c1 = MFMA16(kf[ks], qf[1][ks], c1);
      }
      sacc[t][0] = c0; sacc[t][1] = c1;
      SB(); if constexpr (t < 7) dma_hi(IC(s + 2)); SB();
    });
  }
  STAMP(32);
  constexpr int ST_M = 7 * 8192, ST_L = 4 * 8192;
#pragma unroll
  for (int j = 0; j < 2; ++j) {
    float m = fmaxf(fmaxf(sacc[7][j][0], sacc[7][j][1]), fmaxf(sacc[7][j][2], sacc[7][j][3]));
    m = xmax16_32(fmaxf(mrun[j], m));
    if (g4 == 0) *(LAS float*)(lds + ST_M + ((32 * wh + 16 * j + r16) * 4 + wq2) * 4) = m;
  }
  bar();
  STAMP(33);
  float mc[2], lsum[2] = {0.f, 0.f};
#pragma unroll
  for (int j = 0; j < 2; ++j) {
    f32x4 a = *(LAS f32x4*)(lds + ST_M + (32 * wh + 16 * j + r16) * 16);
    mc[j] = -SM_C * fmaxf(fmaxf(a[0], a[1]), fmaxf(a[2], a[3]));
  }
  LAS char* pb = lds + (32 * wh + r16) * 128 + (((2 * wq2 + (g4 >> 1)) ^ (r16 >> 1)) << 4) + (g4 & 1) * 8;
  auto p_write = [&](auto tc) {
    constexpr int t = decltype(tc)::value;
#pragma unroll
    for (int j = 0; j < 2; ++j) {
      float p[4];
#pragma unroll
      for (int e = 0; e < 4; ++e) p[e] = __builtin_amdgcn_exp2f(fmaf(sacc[t][j][e], SM_C, mc[j]));
      lsum[j] += (p[0] + p[1]) + (p[2] + p[3]);
      u32x2 v = {pk2(p[0], p[1]), pk2(p[2], p[3])};
      *(LAS u32x2*)(pb + t * 8192 + j * 2048) = v;
    }
  };
  p_write(IC(0));
  SB(); dma_hi(IC(16)); SB();
  STAMP(34);

#pragma unroll
  for (int i = 0; i < 16; ++i) { acc[0][i] = 0.f; acc[1][i] = 0.f; }
  float linv[2];
  sfor<8>([&](auto tc) {
    constexpr int t = decltype(tc)::value, s = 15 + t;
    SB(); vmwait<4>(); bar(); STAMP(1 + s); SB();
    Frag f;
#pragma unroll
    for (int ks = 0; ks < 4; ++ks) tn_read(s, t, ks, f);
    f32x4 lp[2];
    if constexpr (t == 7) {
#pragma unroll
      for (int j = 0; j < 2; ++j) lp[j] = *(LAS f32x4*)(lds + ST_L + (32 * j + r32) * 16);
    }
    SB(); if constexpr (t < 7) dma_lo(IC(s + 2)); SB();
#pragma unroll
    for (int ks = 0; ks < 4; ++ks) tn_mma(ks, f, acc);
    if constexpr (t < 7) p_write(IC(t + 1));
    if constexpr (t == 6) {
#pragma unroll
      for (int j = 0; j < 2; ++j) {
        float v = xsum16_32(lsum[j]);
        if (g4 == 0) *(LAS float*)(lds + ST_L + ((32 * wh + 16 * j + r16) * 4 + wq2) * 4) = v;
      }
    }
    if constexpr (t == 7) {
#pragma unroll
      for (int j = 0; j < 2; ++j) linv[j] = 1.0f / ((lp[j][0] + lp[j][1]) + (lp[j][2] + lp[j][3]));
    }
    SB(); if constexpr (t < 7) dma_hi(IC(s + 2)); else dma_lo(IC(s + 2)); SB();
  });
#pragma unroll
  for (int i = 0; i < 16; ++i) { acc[0][i] *= linv[0]; acc[1][i] *= linv[1]; }
  img_write(acc);
  SB(); dma_hi(IC(24)); SB();

  {
    bf16x8 of[2][8];
    bf16x8 fw[2][8];
    const int aWo = (8 * g4 + tq) * 128 + (((2 * wq2 + (tp >> 1)) ^ (((tq >> 1) | ((g4 & 1) << 1)) << 1)) << 4) + (tp & 1) * 8;
    const int voP = (2 * w + hh) * (7 * 8192) + h * 512 + r32 * 16;
    f32x4 c0, c1;
    auto e_mma = [&](int t, int k0, int k1) {
#pragma unroll
      for (int ks = k0; ks < k1; ++ks) {
        if (ks == 0) { c0 = f32x4{0.f, 0.f, 0.f, 0.f}; c1 = c0; }
        c0 = MFMA16(fw[t & 1][ks], of[0][ks], c0); c1 = MFMA16(fw[t & 1][ks], of[1][ks], c1);
      }
    };
    auto e_store = [&](int t) {
      u32x4 v = {pk2(c0[0], c0[1]), pk2(c0[2], c0[3]), pk2(c1[0], c1[1]), pk2(c1[2], c1[3])};
      int j = t + rot; if (j >= 7) j -= 7;
      __builtin_amdgcn_raw_buffer_store_b128(v, rsP, voP, j * 8192, PART_AUX);
    };
    sfor<7>([&](auto tc) {
      constexpr int t = decltype(tc)::value, s = 23 + t;
      constexpr int n = (s + 1 < NTILE ? 4 : 0) + (t >= 2 ? 1 : 0) + (t >= 3 ? 1 : 0);
      if constexpr (t == 0) STAMP(35);
      SB(); vmwait<n>(); bar(); STAMP(1 + s); SB();
      LAS char* base = lds + RING + (s % 3) * SLOT + aWo;
      auto rd = [&](int k0, int k1) {
#pragma unroll
        for (int ks = k0; ks < k1; ++ks) fw[t & 1][ks] = ldstr(base + ks * 4096, base + ks * 4096 + 512);
      };
      if constexpr (t == 0) {
        rd(0, 8); img_read(of); SB();
        dma_lo(IC(s + 2)); dma_hi(IC(s + 2)); SB();
      } else {
        e_mma(t - 1, 0, 2); SB(); rd(0, 3); SB(); dma(IC(s + 2), IC(0)); SB();
        e_mma(t - 1, 2, 4); SB(); rd(3, 6); SB(); dma(IC(s + 2), IC(1)); SB();
        e_mma(t - 1, 4, 6); SB(); rd(6, 8); SB(); dma(IC(s + 2), IC(2)); SB();
        e_mma(t - 1, 6, 8); SB(); dma(IC(s + 2), IC(3)); SB();
        e_store(t - 1); SB();
      }
    });
    e_mma(6, 0, 8); e_store(6);
  }
  STAMP(36);
  vmwait<0>();
  STAMP(37);
  GAS u64* flags = (GAS u64*)(part + FLAG_OFF) + b * 128;
  if (l == 0) __hip_atomic_store(flags + w * 16 + h, tag, RLX_AGENT);
  if (w < 7) {
    GAS u64* fp = flags + (h >> 1) * 16 + r16;
    for (int spins = 0; spins < (1 << 16); ++spins) {
      const u64 v = __hip_atomic_load(fp, RLX_AGENT);
      if (__all(v == tag)) break;
      __builtin_amdgcn_s_sleep(1);
    }
    asm volatile("" ::: "memory");
    STAMP(38);
    u32x4 v[8];
#pragma unroll
    for (int u = 0; u < 8; ++u) v[u] = __builtin_amdgcn_raw_buffer_load_b128(rsP, l * 16 + u * 1024, (h * 7 + w) * 8192, 16);
    if (h == 0 && w == 0 && l == 0) __hip_atomic_store((GAS u64*)(part + EPOCH_OFF + b * 128), tag, RLX_AGENT);
    float sm[8];
#pragma unroll
    for (int e = 0; e < 8; ++e) sm[e] = 0.f;
#pragma unroll
    for (int u = 0; u < 8; ++u)
#pragma unroll
      for (int e = 0; e < 4; ++e) { sm[2 * e] += __uint_as_float(v[u][e] << 16); sm[2 * e + 1] += __uint_as_float(v[u][e] & 0xffff0000u); }
    float o[4];
#pragma unroll
    for (int e = 0; e < 4; ++e) {
      auto x = __builtin_amdgcn_permlane32_swap(__float_as_uint(sm[e]), __float_as_uint(sm[4 + e]), false, false);
      o[e] = __uint_as_float(x[0]) + __uint_as_float(x[1]);
    }
    const int wp = h >> 1, nq = wp >> 1, qh = wp & 1;
    const int q = 32 * qh + 16 * hh + r16, n = 64 * w + 16 * nq + 4 * (2 * (h & 1) + (r32 >> 4));
    u32x2 r = {pk2(o[0], o[1]), pk2(o[2], o[3])};
    *(GAS u32x2*)(out + ((size_t)(b * 64 + q) * DM + n) * 2) = r;
  }
}

extern "C" void kernel_launch(void* const* d_in, const int* in_sizes, int n_in, void* d_out, int out_size, void* d_ws, size_t ws_size,
                              hipStream_t stream) {
  (void)hipFuncSetAttribute((const void*)gqa_main, hipFuncAttributeMaxDynamicSharedMemorySize, LDS_BYTES);
  gqa_main<<<256, 512, LDS_BYTES, stream>>>((const char*)d_in[0], (const char*)d_in[1], (const char*)d_in[2], (const char*)d_in[3],
                                            (const char*)d_in[4], (char*)d_ws, (char*)d_out);
}
```

```cpp
#include <hip/hip_runtime.h>
#include <hip/hip_bf16.h>
#include <utility>

#define GAS __attribute__((address_space(1)))
#define LAS __attribute__((address_space(3)))
#define DI __device__ __forceinline__
typedef short bf16x8 __attribute__((ext_vector_type(8)));
typedef short s16x4 __attribute__((ext_vector_type(4)));
typedef float f32x4 __attribute__((ext_vector_type(4)));
typedef float f32x2 __attribute__((ext_vector_type(2)));
typedef float f32x16 __attribute__((ext_vector_type(16)));
typedef unsigned u32x4 __attribute__((ext_vector_type(4)));
typedef unsigned u32x2 __attribute__((ext_vector_type(2)));
typedef __bf16 bf16x2v __attribute__((ext_vector_type(2)));

template <int N> DI void vmwait() { asm volatile("s_waitcnt vmcnt(%0)" ::"i"(N) : "memory"); }
#define SB() __builtin_amdgcn_sched_barrier(0)
DI void bar() {
  asm volatile("" ::: "memory");
  __builtin_amdgcn_s_waitcnt(0xC07F);
  __builtin_amdgcn_s_barrier();
  asm volatile("" ::: "memory");
}
DI unsigned long long load_epoch(const char* p) {
  unsigned long long v;
  asm volatile("s_load_dwordx2 %0, %1, 0x0\n\ts_waitcnt lgkmcnt(0)" : "=s"(v) : "s"(p) : "memory");
  return v;
}
DI float pin(float v) { asm volatile("" : "+v"(v)); return v; }
DI int pini(int v) { asm volatile("" : "+v"(v)); return v; }
DI void pin4(f32x4& v) { asm volatile("" : "+v"(v)); }
DI void pin2(u32x2& v) { asm volatile("" : "+v"(v)); }
DI unsigned opq_s(unsigned c) { asm volatile("" : "+s"(c)); return c; }
template <int C> DI LAS char* addc(unsigned base) {
  unsigned c = C;
  asm volatile("" : "+s"(c));
  return (LAS char*)(size_t)(base + c);
}
DI float xmax16_32(float m) {
  { auto x = __builtin_amdgcn_permlane16_swap(__float_as_uint(m), __float_as_uint(m), false, false); m = fmaxf(__uint_as_float(x[0]), __uint_as_float(x[1])); }
  { auto x = __builtin_amdgcn_permlane32_swap(__float_as_uint(m), __float_as_uint(m), false, false); m = fmaxf(__uint_as_float(x[0]), __uint_as_float(x[1])); }
  return m;
}
DI float xsum16_32(float m) {
  { auto x = __builtin_amdgcn_permlane16_swap(__float_as_uint(m), __float_as_uint(m), false, false); m = __uint_as_float(x[0]) + __uint_as_float(x[1]); }
  { auto x = __builtin_amdgcn_permlane32_swap(__float_as_uint(m), __float_as_uint(m), false, false); m = __uint_as_float(x[0]) + __uint_as_float(x[1]); }
  return m;
}
DI unsigned pk2(float a, float b) { f32x2 v = {a, b}; return __builtin_bit_cast(unsigned, __builtin_convertvector(v, bf16x2v)); }
DI bf16x8 lds128(LAS char* p) { return *(LAS bf16x8*)p; }
DI bf16x8 ldstr(LAS char* p0, LAS char* p1) {
  s16x4 a = __builtin_amdgcn_ds_read_tr16_b64_v4i16((LAS s16x4*)p0);
  s16x4 b = __builtin_amdgcn_ds_read_tr16_b64_v4i16((LAS s16x4*)p1);
  return __builtin_shufflevector(a, b, 0, 1, 2, 3, 4, 5, 6, 7);
}
template <int C> DI void dma16x(u32x4 rs, unsigned ldsaddr, int voff, int sbase) {
  int so;
  asm volatile("s_mov_b32 m0, %1\n\ts_xor_b32 %0, %4, %5\n\tbuffer_load_dwordx4 %2, %3, %0 offen lds" : "=&s"(so) : "s"(ldsaddr), "v"(voff), "s"(rs), "s"(sbase), "n"(C) : "memory", "scc");
}
template <int C> DI void dma16a(u32x4 rs, unsigned ldsaddr, int voff, int sbase) {
  int so;
  asm volatile("s_mov_b32 m0, %1\n\ts_add_u32 %0, %4, %5\n\tbuffer_load_dwordx4 %2, %3, %0 offen lds" : "=&s"(so) : "s"(ldsaddr), "v"(voff), "s"(rs), "s"(sbase), "n"(C) : "memory", "scc");
}
DI u32x4 mk_rsrc(const char* p, unsigned bytes) {
  unsigned long long a = (unsigned long long)p;
  u32x4 r = {(unsigned)a, (unsigned)(a >> 32) & 0xffffu, bytes, 0x00020000u};
  return r;
}
#define MFMA32(a, b, c) __builtin_amdgcn_mfma_f32_32x32x16_bf16((a), (b), (c), 0, 0, 0)
#define MFMA16(a, b, c) __builtin_amdgcn_mfma_f32_16x16x32_bf16((a), (b), (c), 0, 0, 0)

constexpr int NB = 16, SQ = 64, DM = 448, HQ = 16, HKV = 8, SK = 512, DH = 256;
constexpr int RING = 65536, SLOT = 32768, LDS_BYTES = 163840;
constexpr int NTILE = 30;
constexpr int UT = 10;
constexpr int slot_off(int s) { return s < UT ? RING + (s % 3) * SLOT : ((s - UT) % 4 == 0 ? 32768 : RING + ((UT - 4 + (s - UT) % 4) % 3) * SLOT); }
#define KPOS 8, 6, 0, 4, 1, 2, 5, 7, 3
#define KC 5, 23, 24, 9, 30, 18, 11
constexpr int kpos(int i) { constexpr int t[9] = {KPOS}; return t[i]; }
constexpr int kperm(int p) { int r = 0; for (int i = 0; i < 5; ++i) if ((p >> i) & 1) r |= 512 << kpos(4 + i); return r; }
constexpr int kc(int i) { constexpr int t[7] = {KC}; return kperm(t[i]); }
constexpr int x_off(int p) { return ((p + 4) & 7) * 8192; }
constexpr size_t PART_STRIDE = 16 * 7 * 8192 + 36864, FLAG_OFF = 16 * 7 * 8192, EPOCH_OFF = FLAG_OFF + 32768;
typedef unsigned long long u64;
#define RLX_AGENT __ATOMIC_RELAXED, __HIP_MEMORY_SCOPE_AGENT
constexpr float SM_C = 0.0625f * 1.4426950408889634f;
constexpr int AUX_SC1 = 16, AUX_SC1_NT = 18;
constexpr int PL_C = 7 * 8192, PL_T = 8192, PL_H = 512;
#define ROLE_OF(bid) (((bid) >> 3) & 1)
#define BATCH_OF(bid) ((bid) >> 4)

__global__ __launch_bounds__(768, 3) void gqa_main(const char* __restrict__ X, const char* __restrict__ Kc, const char* __restrict__ Vc,
                                                  const char* __restrict__ WQ, const char* __restrict__ WO, char* __restrict__ part, char* __restrict__ out) {
  extern __shared__ __attribute__((aligned(16))) char smem[];
  LAS char* lds = (LAS char*)smem;
  const unsigned lds0 = (unsigned)(size_t)lds;
#define IC(n) std::integral_constant<int, (n)>{}
  const int bid = blockIdx.x, tid = threadIdx.x;
  const int w = __builtin_amdgcn_readfirstlane(tid >> 6);
  const int l = tid & 63;
  const int xcd = bid & 7, b = BATCH_OF(bid), role = ROLE_OF(bid), h = xcd * 2 + role;
  const int rot = (b * 7) >> 4;
  if (w >= 8) {
    const int lw = w & 3;
    const int kv = b * 8 + xcd;
    const u32x4 rsX = mk_rsrc(X + (size_t)b * (SQ * DM * 2), SQ * DM * 2);
    const u32x4 rsQ = mk_rsrc(WQ + h * 512, DM * HQ * DH * 2 - h * 512);
    u32x4 rsK, rsV, rsO;
    const int drow = 2 * lw + (l >> 5), dpos = l & 31;
    const int orow = 8 * lw + (l >> 3), opos = l & 7;
    const int voQ = drow * 8192 + ((dpos ^ ((drow & 3) << 2)) << 4);
    const int voX = orow * 896 + ((opos ^ ((orow >> 1) & 7)) << 4);
    int kxor, voK, voK2, voV, voO;
    auto late_setup = [&]() {
      const int kv2 = opq_s(kv), h2 = opq_s(h), dr = pini(drow), orw = pini(orow);
      rsK = mk_rsrc(Kc + (size_t)kv2 * (SK * DH * 2), SK * DH * 2);
      rsV = mk_rsrc(Vc + (size_t)kv2 * (SK * DH * 2), SK * DH * 2);
      rsO = mk_rsrc(WO + (size_t)h2 * (DH * DM * 2), DH * DM * 2);
      kxor = ((kv2 & 1) ? kc(0) : 0) ^ ((kv2 & 2) ? kc(1) : 0) ^ ((kv2 & 4) ? kc(2) : 0) ^ ((kv2 & 8) ? kc(3) : 0) ^ ((kv2 & 16) ? kc(4) : 0) ^
             ((kv2 & 32) ? kc(5) : 0) ^ ((kv2 & 64) ? kc(6) : 0);
      const int krow = ((dr & 1) << (9 + kpos(0))) | (((dr >> 1) & 1) << (9 + kpos(1))) | (((dr >> 2) & 1) << (9 + kpos(2)));
      voK = krow + ((dpos ^ (dr & 15)) << 4); voK2 = voK ^ 128;
      voV = krow + ((dpos ^ ((dr & 3) << 2)) << 4);
      voO = orw * 896 + ((opos ^ ((((orw >> 1) & 1) | (((orw >> 3) & 1) << 1)) << 1)) << 4);
    };
    auto dma = [&](auto sc, auto ic) {
      constexpr int s = decltype(sc)::value, i = decltype(ic)::value;
      const unsigned dst = lds0 + slot_off(s) + i * 8192 + lw * 1024;
      if constexpr (s < 7) {
        int j = s + rot; if (j >= 7) j -= 7;
        dma16a<i * 16 * 8192>(rsQ, dst, voQ, j * 64 * 8192); dma16a<(i * 16 + 8) * 8192>(rsQ, dst + 4096, voQ, j * 64 * 8192);
      } else if constexpr (s < 15) {
        constexpr int c = kperm(4 * (s - 7) + i);
        dma16x<c>(rsK, dst, voK, kxor); dma16x<c + (512 << kpos(3))>(rsK, dst + 4096, voK2, kxor);
      } else if constexpr (s < 23) {
        constexpr int c = kperm(4 * (s - 15) + i);
        dma16x<c>(rsV, dst, voV, kxor); dma16x<c + (512 << kpos(3))>(rsV, dst + 4096, voV, kxor);
      } else if constexpr (s < NTILE) {
        int j = s - 23 + rot; if (j >= 7) j -= 7;
        dma16a<i * 64 * 896>(rsO, dst, voO, j * 128); dma16a<(i * 64 + 32) * 896>(rsO, dst + 4096, voO, j * 128);
      }
    };
    auto dma_x = [&](int p) {
      int j = p + rot; if (j >= 7) j -= 7;
      const unsigned dst = lds0 + ((p + 4) & 7) * 8192 + lw * 1024;
      dma16a<0>(rsX, dst, voX, j * 128); dma16a<32 * 896>(rsX, dst + 4096, voX, j * 128);
    };
    auto dma_xr = [&](int i, int base, int n, int shift) { int p = i + role * shift; if (p >= n) p -= n; dma_x(base + p); };
    __builtin_amdgcn_s_sleep(4); late_setup(); dma_x(1); dma(IC(1), IC(0)); dma(IC(1), IC(1)); dma(IC(1), IC(2)); dma(IC(1), IC(3));
    __builtin_amdgcn_s_barrier();
    dma_x(2); dma_xr(0, 3, 4, 2); dma_xr(1, 3, 4, 2); dma(IC(2), IC(0)); dma(IC(2), IC(1)); dma(IC(2), IC(2)); dma(IC(2), IC(3));
    vmwait<14>(); __builtin_amdgcn_s_barrier();
    dma_xr(2, 3, 4, 2); dma_xr(3, 3, 4, 2); dma(IC(3), IC(0)); dma(IC(3), IC(1)); dma(IC(3), IC(2)); dma(IC(3), IC(3));
    vmwait<12>(); __builtin_amdgcn_s_barrier();
    dma(IC(4), IC(0)); dma(IC(4), IC(1)); dma(IC(4), IC(2)); dma(IC(4), IC(3)); vmwait<8>(); __builtin_amdgcn_s_barrier();
    dma(IC(5), IC(0)); dma(IC(5), IC(1)); dma(IC(5), IC(2)); dma(IC(5), IC(3)); vmwait<8>(); __builtin_amdgcn_s_barrier();
    dma(IC(6), IC(0)); dma(IC(6), IC(1)); dma(IC(6), IC(2)); dma(IC(6), IC(3)); vmwait<8>(); __builtin_amdgcn_s_barrier();
    dma(IC(7), IC(0)); dma(IC(7), IC(1)); dma(IC(7), IC(2)); dma(IC(7), IC(3)); vmwait<8>(); __builtin_amdgcn_s_barrier();
    dma(IC(8), IC(0)); dma(IC(8), IC(1)); dma(IC(8), IC(2)); dma(IC(8), IC(3)); vmwait<8>(); __builtin_amdgcn_s_barrier();
    dma(IC(9), IC(0)); dma(IC(9), IC(1)); dma(IC(9), IC(2)); __builtin_amdgcn_s_barrier();
    dma(IC(9), IC(3)); dma(IC(10), IC(0)); dma(IC(10), IC(1)); dma(IC(10), IC(2)); vmwait<14>(); __builtin_amdgcn_s_barrier();
    dma(IC(10), IC(3)); dma(IC(11), IC(0)); dma(IC(11), IC(1)); dma(IC(11), IC(2)); vmwait<14>(); __builtin_amdgcn_s_barrier();
    dma(IC(11), IC(3)); dma(IC(12), IC(0)); dma(IC(12), IC(1)); dma(IC(12), IC(2)); vmwait<14>(); __builtin_amdgcn_s_barrier();
    dma(IC(12), IC(3)); dma(IC(13), IC(0)); dma(IC(13), IC(1)); dma(IC(13), IC(2)); vmwait<14>(); __builtin_amdgcn_s_barrier();
    dma(IC(13), IC(3)); dma(IC(14), IC(0)); dma(IC(14), IC(1)); vmwait<12>(); __builtin_amdgcn_s_barrier();
    dma(IC(14), IC(2)); dma(IC(14), IC(3)); dma(IC(15), IC(0)); dma(IC(15), IC(1)); vmwait<12>(); __builtin_amdgcn_s_barrier();
    dma(IC(15), IC(2)); dma(IC(15), IC(3)); dma(IC(16), IC(0)); dma(IC(16), IC(1)); vmwait<12>(); __builtin_amdgcn_s_barrier();
    dma(IC(16), IC(2)); dma(IC(16), IC(3)); dma(IC(17), IC(0)); dma(IC(17), IC(1)); vmwait<12>(); __builtin_amdgcn_s_barrier();
    dma(IC(17), IC(2)); dma(IC(17), IC(3)); dma(IC(18), IC(0)); dma(IC(18), IC(1)); vmwait<12>(); __builtin_amdgcn_s_barrier();
    dma(IC(18), IC(2)); dma(IC(18), IC(3)); dma(IC(19), IC(0)); dma(IC(19), IC(1)); vmwait<12>(); __builtin_amdgcn_s_barrier();
    dma(IC(19), IC(2)); dma(IC(19), IC(3)); dma(IC(20), IC(0)); dma(IC(20), IC(1)); vmwait<12>(); __builtin_amdgcn_s_barrier();
    dma(IC(20), IC(2)); dma(IC(20), IC(3)); dma(IC(21), IC(0)); dma(IC(21), IC(1)); vmwait<12>(); __builtin_amdgcn_s_barrier();
    dma(IC(21), IC(2)); dma(IC(21), IC(3)); dma(IC(22), IC(0)); dma(IC(22), IC(1)); vmwait<12>(); __builtin_amdgcn_s_barrier();
    dma(IC(22), IC(2)); dma(IC(22), IC(3)); dma(IC(23), IC(0)); vmwait<10>(); __builtin_amdgcn_s_barrier();
    dma(IC(23), IC(1)); dma(IC(23), IC(2)); dma(IC(23), IC(3)); vmwait<12>(); __builtin_amdgcn_s_barrier();
    dma(IC(24), IC(0)); dma(IC(24), IC(1)); vmwait<12>(); __builtin_amdgcn_s_barrier();
    __builtin_amdgcn_s_sleep(8); dma(IC(24), IC(2)); dma(IC(24), IC(3)); vmwait<8>(); __builtin_amdgcn_s_barrier();
    dma(IC(25), IC(0)); dma(IC(25), IC(1)); dma(IC(25), IC(2)); dma(IC(25), IC(3)); dma(IC(26), IC(0)); dma(IC(26), IC(1)); dma(IC(26), IC(2));
    dma(IC(26), IC(3)); vmwait<16>(); __builtin_amdgcn_s_barrier();
    dma(IC(27), IC(0)); dma(IC(27), IC(1)); dma(IC(27), IC(2)); dma(IC(27), IC(3)); vmwait<16>(); __builtin_amdgcn_s_barrier();
    dma(IC(28), IC(0)); dma(IC(28), IC(1)); dma(IC(28), IC(2)); dma(IC(28), IC(3)); vmwait<16>(); __builtin_amdgcn_s_barrier();
    dma(IC(29), IC(0)); dma(IC(29), IC(1)); dma(IC(29), IC(2)); dma(IC(29), IC(3)); vmwait<16>(); __builtin_amdgcn_s_barrier();
    vmwait<8>(); __builtin_amdgcn_s_barrier();
    vmwait<0>(); __builtin_amdgcn_s_barrier();
    return;
  }
  {
    const u32x4 rsX = mk_rsrc(X + (size_t)b * (SQ * DM * 2), SQ * DM * 2);
    const u32x4 rsQ = mk_rsrc(WQ + h * 512, DM * HQ * DH * 2 - h * 512);
    const int drow = 2 * w + (l >> 5), dpos = l & 31, orow = 8 * w + (l >> 3), opos = l & 7;
    const int voQ = drow * 8192 + ((dpos ^ ((drow & 3) << 2)) << 4);
    const int voX = orow * 896 + ((opos ^ ((orow >> 1) & 7)) << 4);
    auto dma = [&](auto sc, auto ic) {
      constexpr int s = decltype(sc)::value, i = decltype(ic)::value;
      int j = s + rot; if (j >= 7) j -= 7;
      dma16a<i * 16 * 8192>(rsQ, lds0 + slot_off(s) + i * 8192 + w * 1024, voQ, j * 64 * 8192);
    };
    auto dma_x = [&](int p) {
      int j = p + rot; if (j >= 7) j -= 7;
      dma16a<0>(rsX, lds0 + ((p + 4) & 7) * 8192 + w * 1024, voX, j * 128);
    };
    dma_x(0); dma(IC(0), IC(0)); dma(IC(0), IC(1)); dma(IC(0), IC(2)); dma(IC(0), IC(3));
  }
  auto rsP = __builtin_amdgcn_make_buffer_rsrc((void*)(part + (size_t)b * PART_STRIDE), 0, 16 * 7 * 8192, 0x00020000);
  u64 tag;
  auto get_tag = [&]() { tag = load_epoch(part + (size_t)b * PART_STRIDE + EPOCH_OFF) * 6364136223846793005ull + 1442695040888963407ull; };
  const int wq2 = w >> 1, wh = w & 1;
  int lb, hh, r32, r16, g4, tq, tp, aTr, aSt[4], aRow[4];
  auto lanes = [&]() {
    lb = __builtin_amdgcn_mbcnt_hi(~0u, __builtin_amdgcn_mbcnt_lo(~0u, opq_s(0)));
    hh = lb >> 5; r32 = lb & 31; r16 = lb & 15; g4 = lb >> 4; tq = (lb & 15) >> 2; tp = lb & 3;
    aTr = (8 * hh + tq) * 512 + (((4 * w + 2 * (g4 & 1) + (tp >> 1)) ^ (tq << 2)) << 4) + (tp & 1) * 8;
#pragma unroll
    for (int s = 0; s < 4; ++s) {
      aSt[s] = r32 * 128 + (((2 * s + hh) ^ ((r32 >> 1) & 7)) << 4);
      aRow[s] = r16 * 512 + (((4 * s + g4) ^ r16) << 4);
    }
  };
  lanes();
  auto lanes2 = [&]() {
    const int x = __builtin_amdgcn_mbcnt_hi(~0u, __builtin_amdgcn_mbcnt_lo(~0u, opq_s(0)));
    aTr = pini((8 * (x >> 5) + ((x & 15) >> 2)) * 512 + (((4 * w + 2 * ((x >> 4) & 1) + ((x & 3) >> 1)) ^ (x & 12)) << 4) + (x & 1) * 8);
#pragma unroll
    for (int s = 0; s < 4; ++s) aSt[s] = pini((x & 31) * 128 + (((2 * s + (x >> 5)) ^ (((x & 31) >> 1) & 7)) << 4));
  };
#define SYNC(n, s) do { SB(); if constexpr ((n) < 63) vmwait<(n)>(); bar(); SB(); } while (0)
  constexpr int ST_M = 3 * 8192, ST_L = 0;
  const f32x4 zero4 = {0.f, 0.f, 0.f, 0.f};
  f32x16 zero16;
#pragma unroll
  for (int i = 0; i < 16; ++i) zero16[i] = 0.f;
  LAS char* tb;
  LAS char* kb[4];
  auto tr_base = [&](auto sc) { tb = addc<slot_off(decltype(sc)::value)>(lds0 + aTr); };
  auto a_read = [&](int ks) { return ldstr(tb + ks * 8192, tb + ks * 8192 + 2048); };

  f32x16 acc[2];
  bf16x8 fa[28], xb[28][2];
  auto wq_rd = [&](int g) { fa[g] = a_read(g & 3); };
  auto x_rd = [&](int g, int j) { xb[g][j] = lds128(lds + aSt[g & 3] + x_off(g >> 2) + j * 4096); };
  auto mm = [&](int g, int j) { acc[j] = MFMA32(fa[g], xb[g][j], g == 0 ? zero16 : acc[j]); };
  auto img_wr = [&](int j, int rg) {
    u32x2 v = {pk2(acc[j][4 * rg], acc[j][4 * rg + 1]), pk2(acc[j][4 * rg + 2], acc[j][4 * rg + 3])};
    *(LAS u32x2*)(lds + r32 * 512 + (((4 * w + rg) ^ r16) << 4) + 8 * hh + j * 16384) = v;
  };
  auto img_rd = [&](int j, int ks) { return lds128(lds + aRow[ks & 3] + wh * 16384 + (ks >> 2) * 256 + j * 8192); };
  bf16x8 kq[8][8], qf[2][8];
  f32x4 sacc[8][2];
  float mrun[2];
  unsigned kslot;
  auto k_base = [&](auto sc, int c) {
    if (c == 0) kslot = opq_s(lds0 + wq2 * 8192 + slot_off(decltype(sc)::value));
    kb[c] = (LAS char*)(size_t)(kslot + aRow[c]);
  };
  auto k_rd = [&](int t, int ks) { kq[t][ks] = lds128(kb[ks & 3] + (ks >> 2) * 256); };
  auto q_rd = [&](int j, int ks) { qf[j][ks] = img_rd(j, ks); };
  auto qk = [&](int t, int ks, int j) { sacc[t][j] = MFMA16(kq[t][ks], qf[j][ks], ks == 0 ? zero4 : sacc[t][j]); };
  auto rmax = [&](int t, int j) {
    const float m = fmaxf(fmaxf(sacc[t][j][0], sacc[t][j][1]), sacc[t][j][2]);
    mrun[j] = fmaxf(fmaxf(m, sacc[t][j][3]), t == 0 ? m : mrun[j]);
  };
#define stw (lds + ((32 * wh + r16) * 4 + wq2) * 4)
#define str (lds + (32 * wh + r16) * 16)
  auto m_publish = [&]() {
    const float m0 = xmax16_32(mrun[0]), m1 = xmax16_32(mrun[1]);
    if (g4 == 0) { *(LAS float*)(stw + ST_M) = m0; *(LAS float*)(stw + ST_M + 256) = m1; }
  };
  float mc[2], lsum[2] = {0.f, 0.f}, linv[2];
  f32x4 sta[2];
  auto m_read = [&]() { sta[0] = *(LAS f32x4*)(str + ST_M); sta[1] = *(LAS f32x4*)(str + ST_M + 256); };
  f32x4 stb[2];
  float md[2];
  auto m_read2 = [&]() { stb[0] = *(LAS f32x4*)(lds + ST_M + r32 * 16); stb[1] = *(LAS f32x4*)(lds + ST_M + r32 * 16 + 512); };
  auto max4 = [&](const f32x4& a) { return -SM_C * fmaxf(fmaxf(fmaxf(a[0], a[1]), a[2]), a[3]); };
  auto m_fin1 = [&]() { mc[0] = pin(max4(sta[0])); mc[1] = pin(max4(sta[1])); };
  auto m_fin1d = [&]() { md[0] = pin(max4(stb[0])); md[1] = pin(max4(stb[1])); };
  auto m_fin2 = [&]() {
#pragma unroll
    for (int j = 0; j < 2; ++j) {
      const float mn = max4(sta[j]);
      lsum[j] = pin(lsum[j] * __builtin_amdgcn_exp2f(mn - mc[j]));
      mc[j] = pin(mn);
    }
  };
  auto m_fin2d = [&]() { md[0] = pin(__builtin_amdgcn_exp2f(max4(stb[0]) - md[0])); md[1] = pin(__builtin_amdgcn_exp2f(max4(stb[1]) - md[1])); };
  auto resc = [&](int j) {
#pragma unroll
    for (int i = 0; i < 16; ++i) acc[j][i] *= md[j];
  };
  auto l_publish = [&]() {
    const float v0 = xsum16_32(lsum[0]), v1 = xsum16_32(lsum[1]);
    if (g4 == 0) { *(LAS float*)(stw + ST_L) = v0; *(LAS float*)(stw + ST_L + 256) = v1; }
  };
  auto l_read = [&]() { sta[0] = *(LAS f32x4*)(lds + ST_L + (32 * wh + 2 * r16) * 16); sta[1] = *(LAS f32x4*)(lds + ST_L + (32 * wh + 2 * r16) * 16 + 16); };
  auto l_finish = [&]() {
#pragma unroll
    for (int j = 0; j < 2; ++j) linv[j] = __builtin_amdgcn_rcpf((sta[j][0] + sta[j][1]) + (sta[j][2] + sta[j][3]));
  };
#define pb (lds + (32 * wh + r16) * 128 + (((2 * wq2 + (g4 >> 1)) ^ (r16 >> 1)) << 4) + (g4 & 1) * 8)
  u32x2 pk[8][2];
  auto s_fm = [&](int t, int j) {
#pragma unroll
    for (int e = 0; e < 4; ++e) sacc[t][j][e] = fmaf(sacc[t][j][e], SM_C, mc[j]);
    pin4(sacc[t][j]);
  };
  auto s_ex = [&](int t, int j, int e0, int e1) {
#pragma unroll
    for (int e = e0; e < e1; ++e) sacc[t][j][e] = __builtin_amdgcn_exp2f(sacc[t][j][e]);
    pin4(sacc[t][j]);
  };
  auto p_pack = [&](int t, int j) {
    const f32x4 p = sacc[t][j];
    lsum[j] += (p[0] + p[1]) + (p[2] + p[3]);
    pk[t][j] = u32x2{pk2(p[0], p[1]), pk2(p[2], p[3])};
    pin2(pk[t][j]); lsum[j] = pin(lsum[j]);
  };
  auto p_wr = [&](int t) { *(LAS u32x2*)(pb + (t & 3) * 8192) = pk[t][0]; *(LAS u32x2*)(pb + (t & 3) * 8192 + 2048) = pk[t][1]; };
  bf16x8 va[8][4], pf[8][4][2];
  auto v_rd = [&](int t, int ks) { va[t][ks] = a_read(ks); };
  auto p_rd = [&](int t, int ks, int j) { pf[t][ks][j] = lds128(lds + aSt[ks] + (t & 3) * 8192 + j * 4096); };
  auto pv = [&](int t, int ks, int j) { acc[j] = MFMA32(va[t][ks], pf[t][ks][j], (t | ks) == 0 ? zero16 : acc[j]); };
  bf16x8 of[2][8], fw[7][8];
  f32x4 cc[7][2];
  u32x4 pkd;
  int aWo, voPs, aO[4];
#define cons (8 * wh + (r16 >> 1))
#define pin_off (wq2 * 128 + g4 * 32 + (r16 & 1) * 16)
#define voP (cons * PL_C + h * PL_H + pin_off)
  GAS u64* flags = (GAS u64*)(part + (size_t)b * PART_STRIDE + FLAG_OFF);
  LAS char* wb[7];
  auto wo_rd = [&](int t, int ks) { fw[t][ks] = ldstr(wb[t] + ks * 4096, wb[t] + ks * 4096 + 512); };
  auto wo_base = [&](auto sc) { wb[decltype(sc)::value - 23] = addc<slot_off(decltype(sc)::value)>(lds0 + aWo); };
  auto o_rd = [&](int j, int ks) { of[j][ks] = img_rd(j, ks); };
  auto em = [&](int t, int ks, int j) { cc[t][j] = MFMA16(fw[t][ks], of[j][ks], ks == 0 ? zero4 : cc[t][j]); };
  auto e_pack = [&](int t, int j) {
    const f32x4 c = cc[t][j];
    pkd[2 * j] = pk2(c[0] * linv[j], c[1] * linv[j]); pkd[2 * j + 1] = pk2(c[2] * linv[j], c[3] * linv[j]);
  };
  auto lanesE = [&]() {
    { const int a = aTr, s0 = aSt[0], s1 = aSt[1], s2 = aSt[2], s3 = aSt[3]; lanes(); aTr = a; aSt[0] = s0; aSt[1] = s1; aSt[2] = s2; aSt[3] = s3; }
    aWo = pini((8 * g4 + tq) * 128 + (((2 * wq2 + (tp >> 1)) ^ (((tq >> 1) | ((g4 & 1) << 1)) << 1)) << 4) + (tp & 1) * 8);
    voPs = pini(cons == h ? 0x7ff00000 : voP);
    const int rho = ((r32 & 1) << 4) | (r32 >> 1);
#pragma unroll
    for (int c = 0; c < 4; ++c) { aO[c] = pini(rho * 512 + (((4 * w + c) ^ (rho & 15)) << 4) + 8 * hh); aRow[c] = pini(aRow[c]); }
  };
  auto img_wr_o = [&](int j, int rg) {
    u32x2 v = {pk2(acc[j][4 * rg], acc[j][4 * rg + 1]), pk2(acc[j][4 * rg + 2], acc[j][4 * rg + 3])};
    *(LAS u32x2*)(lds + aO[rg] + j * 16384) = v;
  };
  auto e_store = [&](int t) {
    __builtin_amdgcn_raw_buffer_store_b128(pkd, rsP, voPs + t * PL_T, 0, AUX_SC1);
    if (cons == h) *(LAS u32x4*)(lds + t * 512 + pin_off) = pkd;
  };
  auto e_flag = [&](int t0, int t1) {
    if (w == 7 && lb < t1 - t0) __hip_atomic_store(flags + (t0 + lb) * 16 + h, tag, RLX_AGENT);
  };
  const int ct = w;
#define fp (flags + ct * 16 + r16)
  u64 fv;
  auto poll0 = [&]() { fv = __hip_atomic_load(fp, RLX_AGENT); };
  u32x4 v[8];
  unsigned done = 0;
  auto v_clear = [&]() {
#pragma unroll
    for (int u = 0; u < 8; ++u) v[u] = u32x4{0u, 0u, 0u, 0u};
  };


  get_tag(); SB();
  SYNC(0, 0);
  x_rd(0, 0); SB(); x_rd(0, 1); SB(); x_rd(1, 0); SB(); x_rd(1, 1); SB(); tr_base(IC(0)); SB(); wq_rd(0); SB(); wq_rd(1); SB(); wq_rd(2); SB();
  wq_rd(3); SB(); x_rd(2, 0); SB(); x_rd(2, 1); SB(); x_rd(3, 0); SB(); x_rd(3, 1); SB(); mm(0, 0); SB(); mm(0, 1); SB(); mm(1, 0); SB();
  mm(1, 1); SB(); mm(2, 0); SB(); mm(2, 1); SB(); mm(3, 0); SB(); mm(3, 1); SB();
  SYNC(63, 1);
  x_rd(4, 0); SB(); x_rd(4, 1); SB(); x_rd(5, 0); SB(); x_rd(5, 1); SB(); tr_base(IC(1)); SB(); wq_rd(4); SB(); wq_rd(5); SB(); wq_rd(6); SB();
  wq_rd(7); SB(); x_rd(6, 0); SB(); x_rd(6, 1); SB(); x_rd(7, 0); SB(); x_rd(7, 1); SB(); mm(4, 0); SB(); mm(4, 1); SB(); mm(5, 0); SB();
  mm(5, 1); SB(); mm(6, 0); SB(); mm(6, 1); SB(); mm(7, 0); SB(); mm(7, 1); SB();
  SYNC(63, 2);
  x_rd(8, 0); SB(); x_rd(8, 1); SB(); x_rd(9, 0); SB(); x_rd(9, 1); SB(); tr_base(IC(2)); SB(); wq_rd(8); SB(); wq_rd(9); SB(); wq_rd(10); SB();
  wq_rd(11); SB(); x_rd(10, 0); SB(); x_rd(10, 1); SB(); x_rd(11, 0); SB(); x_rd(11, 1); SB(); mm(8, 0); SB(); mm(8, 1); SB(); mm(9, 0); SB();
  mm(9, 1); SB(); mm(10, 0); SB(); mm(10, 1); SB(); mm(11, 0); SB(); mm(11, 1); SB();
  SYNC(63, 3);
  x_rd(12, 0); SB(); x_rd(12, 1); SB(); x_rd(13, 0); SB(); x_rd(13, 1); SB(); tr_base(IC(3)); SB(); wq_rd(12); SB(); wq_rd(13); SB(); wq_rd(14); SB();
  wq_rd(15); SB(); mm(12, 0); SB(); x_rd(14, 0); SB(); mm(12, 1); SB(); x_rd(14, 1); SB(); mm(13, 0); SB(); x_rd(15, 0); SB(); mm(13, 1); SB();
  x_rd(15, 1); SB();
  SYNC(63, 4);
  mm(14, 0); SB(); tr_base(IC(4)); SB(); x_rd(16, 0); SB(); wq_rd(16); SB(); mm(14, 1); SB(); x_rd(16, 1); SB(); wq_rd(17); SB(); mm(15, 0); SB();
  x_rd(17, 0); SB(); wq_rd(18); SB(); mm(15, 1); SB(); x_rd(17, 1); SB(); wq_rd(19); SB();
  SYNC(63, 5);
  mm(16, 0); SB(); tr_base(IC(5)); SB(); x_rd(18, 0); SB(); wq_rd(20); SB(); mm(16, 1); SB(); x_rd(18, 1); SB(); wq_rd(21); SB(); mm(17, 0); SB();
  x_rd(19, 0); SB(); wq_rd(22); SB(); mm(17, 1); SB(); x_rd(19, 1); SB(); wq_rd(23); SB();
  SYNC(63, 6);
  mm(18, 0); SB(); x_rd(20, 0); SB(); tr_base(IC(6)); SB(); mm(18, 1); SB(); x_rd(20, 1); SB(); x_rd(21, 0); SB(); mm(19, 0); SB(); x_rd(21, 1); SB();
  mm(19, 1); SB(); wq_rd(24); SB(); mm(20, 0); SB(); x_rd(22, 0); SB(); x_rd(22, 1); SB(); mm(20, 1); SB(); wq_rd(25); SB(); mm(21, 0); SB();
  x_rd(23, 0); SB(); mm(21, 1); SB(); x_rd(23, 1); SB(); wq_rd(26); SB(); mm(22, 0); SB(); x_rd(24, 0); SB(); mm(22, 1); SB(); x_rd(24, 1); SB();
  x_rd(25, 0); SB(); mm(23, 0); SB(); wq_rd(27); SB(); mm(23, 1); SB(); x_rd(25, 1); SB(); mm(24, 0); SB(); x_rd(26, 0); SB(); mm(24, 1); SB();
  x_rd(26, 1); SB();
  SYNC(63, 7);
  mm(25, 0); SB(); k_base(IC(7), 0); SB(); k_rd(0, 0); SB(); k_base(IC(7), 1); SB(); x_rd(27, 0); SB(); k_rd(0, 1); SB(); k_base(IC(7), 2); SB();
  k_rd(0, 2); SB(); mm(25, 1); SB(); k_base(IC(7), 3); SB(); k_rd(0, 3); SB(); k_rd(0, 4); SB(); x_rd(27, 1); SB(); k_rd(0, 5); SB(); k_rd(0, 6); SB();
  k_rd(0, 7); SB(); bar(); SB(); mm(26, 0); SB(); mm(27, 0); SB(); mm(26, 1); SB(); mm(27, 1); SB(); img_wr(0, 0); SB(); img_wr(0, 1); SB();
  img_wr(0, 2); SB(); img_wr(0, 3); SB(); img_wr(1, 0); SB(); img_wr(1, 1); SB(); img_wr(1, 2); SB(); img_wr(1, 3); SB();
  SYNC(63, 8);
  q_rd(0, 0); SB(); q_rd(1, 0); SB(); q_rd(0, 1); SB(); q_rd(1, 1); SB(); q_rd(0, 2); SB(); q_rd(1, 2); SB(); qk(0, 0, 0); SB(); q_rd(0, 3); SB();
  qk(0, 0, 1); SB(); q_rd(1, 3); SB(); qk(0, 1, 0); SB(); q_rd(0, 4); SB(); qk(0, 1, 1); SB(); q_rd(1, 4); SB(); qk(0, 2, 0); SB(); q_rd(0, 5); SB();
  qk(0, 2, 1); SB(); q_rd(1, 5); SB(); qk(0, 3, 0); SB(); q_rd(0, 6); SB(); qk(0, 3, 1); SB(); q_rd(1, 6); SB(); qk(0, 4, 0); SB(); q_rd(0, 7); SB();
  qk(0, 4, 1); SB(); q_rd(1, 7); SB(); qk(0, 5, 0); SB(); k_base(IC(8), 0); SB(); k_rd(1, 0); SB(); qk(0, 5, 1); SB(); k_base(IC(8), 1); SB();
  k_rd(1, 1); SB(); qk(0, 6, 0); SB(); k_base(IC(8), 2); SB(); k_rd(1, 2); SB(); qk(0, 6, 1); SB(); k_base(IC(8), 3); SB(); k_rd(1, 3); SB();
  qk(0, 7, 0); SB(); k_rd(1, 4); SB(); k_rd(1, 5); SB(); qk(0, 7, 1); SB(); k_rd(1, 6); SB(); k_rd(1, 7); SB();
  SYNC(63, 9);
  qk(1, 0, 0); SB(); k_base(IC(9), 0); SB(); qk(1, 0, 1); SB(); k_rd(2, 0); SB(); qk(1, 1, 0); SB(); k_base(IC(9), 1); SB(); qk(1, 1, 1); SB();
  rmax(0, 0); SB(); qk(1, 2, 0); SB(); qk(1, 2, 1); SB(); k_rd(2, 1); SB(); qk(1, 3, 0); SB(); k_base(IC(9), 2); SB(); qk(1, 3, 1); SB();
  k_rd(2, 2); SB(); qk(1, 4, 0); SB(); k_base(IC(9), 3); SB(); qk(1, 4, 1); SB(); k_rd(2, 3); SB(); qk(1, 5, 0); SB(); k_rd(2, 4); SB();
  qk(1, 5, 1); SB(); rmax(0, 1); SB(); qk(1, 6, 0); SB(); qk(1, 6, 1); SB(); k_rd(2, 5); SB(); qk(1, 7, 0); SB(); k_rd(2, 6); SB(); qk(1, 7, 1); SB();
  k_rd(2, 7); SB(); qk(2, 0, 0); SB(); qk(2, 0, 1); SB(); qk(2, 1, 0); SB(); qk(2, 1, 1); SB(); qk(2, 2, 0); SB(); qk(2, 2, 1); SB();
  qk(2, 3, 0); SB(); qk(2, 3, 1); SB();
  SYNC(63, 10);
  qk(2, 4, 0); SB(); k_base(IC(10), 0); SB(); k_rd(3, 0); SB(); qk(2, 4, 1); SB(); k_base(IC(10), 1); SB(); qk(2, 5, 0); SB(); k_rd(3, 1); SB();
  rmax(1, 0); SB(); qk(2, 5, 1); SB(); k_base(IC(10), 2); SB(); k_rd(3, 2); SB(); rmax(1, 1); SB(); qk(2, 6, 0); SB(); k_base(IC(10), 3); SB();
  qk(2, 6, 1); SB(); k_rd(3, 3); SB(); qk(2, 7, 0); SB(); qk(2, 7, 1); SB(); k_rd(3, 4); SB(); qk(3, 0, 0); SB(); qk(3, 0, 1); SB(); k_rd(3, 5); SB();
  qk(3, 1, 0); SB(); qk(3, 1, 1); SB(); k_rd(3, 6); SB(); qk(3, 2, 0); SB(); rmax(2, 0); SB(); qk(3, 2, 1); SB(); k_rd(3, 7); SB(); rmax(2, 1); SB();
  qk(3, 3, 0); SB(); qk(3, 3, 1); SB(); qk(3, 4, 0); SB(); qk(3, 4, 1); SB(); qk(3, 5, 0); SB(); qk(3, 5, 1); SB(); qk(3, 6, 0); SB();
  qk(3, 6, 1); SB(); qk(3, 7, 0); SB(); qk(3, 7, 1); SB(); rmax(3, 0); SB(); rmax(3, 1); SB(); m_publish(); SB();
  SYNC(63, 11);
  m_read(); SB(); k_base(IC(11), 0); SB(); k_rd(4, 0); SB(); k_base(IC(11), 1); SB(); k_rd(4, 1); SB(); k_base(IC(11), 2); SB(); k_rd(4, 2); SB();
  k_base(IC(11), 3); SB(); m_fin1(); SB(); k_rd(4, 3); SB(); s_fm(0, 0); SB(); s_fm(0, 1); SB(); k_rd(4, 4); SB(); qk(4, 0, 0); SB();
  s_ex(0, 0, 0, 2); SB(); qk(4, 0, 1); SB(); k_rd(4, 5); SB(); qk(4, 1, 0); SB(); s_ex(0, 0, 2, 4); SB(); qk(4, 1, 1); SB(); k_rd(4, 6); SB();
  s_ex(0, 1, 0, 2); SB(); qk(4, 2, 0); SB(); lanes2(); SB(); s_ex(0, 1, 2, 4); SB(); qk(4, 2, 1); SB(); k_rd(4, 7); SB(); p_pack(0, 0); SB();
  qk(4, 3, 0); SB(); qk(4, 3, 1); SB(); p_pack(0, 1); SB();
  SYNC(63, 12);
  qk(4, 4, 0); SB(); p_wr(0); SB(); k_base(IC(12), 0); SB(); k_rd(5, 0); SB(); qk(4, 4, 1); SB(); k_base(IC(12), 1); SB(); qk(4, 5, 0); SB();
  k_rd(5, 1); SB(); qk(4, 5, 1); SB(); k_base(IC(12), 2); SB(); k_rd(5, 2); SB(); qk(4, 6, 0); SB(); k_base(IC(12), 3); SB(); qk(4, 6, 1); SB();
  k_rd(5, 3); SB(); s_fm(1, 0); SB(); qk(4, 7, 0); SB(); s_fm(1, 1); SB(); qk(4, 7, 1); SB(); k_rd(5, 4); SB(); qk(5, 0, 0); SB();
  s_ex(1, 0, 0, 2); SB(); qk(5, 0, 1); SB(); k_rd(5, 5); SB(); qk(5, 1, 0); SB(); s_ex(1, 0, 2, 4); SB(); qk(5, 1, 1); SB(); k_rd(5, 6); SB();
  s_ex(1, 1, 0, 2); SB(); qk(5, 2, 0); SB(); s_ex(1, 1, 2, 4); SB(); qk(5, 2, 1); SB(); k_rd(5, 7); SB(); p_pack(1, 0); SB(); qk(5, 3, 0); SB();
  qk(5, 3, 1); SB(); p_pack(1, 1); SB();
  SYNC(63, 13);
  qk(5, 4, 0); SB(); p_wr(1); SB(); m_read2(); SB(); k_base(IC(13), 0); SB(); k_rd(6, 0); SB(); qk(5, 4, 1); SB(); k_base(IC(13), 1); SB();
  qk(5, 5, 0); SB(); k_rd(6, 1); SB(); rmax(4, 0); SB(); qk(5, 5, 1); SB(); k_base(IC(13), 2); SB(); k_rd(6, 2); SB(); rmax(4, 1); SB();
  qk(5, 6, 0); SB(); m_fin1d(); SB(); k_base(IC(13), 3); SB(); qk(5, 6, 1); SB(); k_rd(6, 3); SB(); s_fm(2, 0); SB(); qk(5, 7, 0); SB();
  s_fm(2, 1); SB(); qk(5, 7, 1); SB(); k_rd(6, 4); SB(); qk(6, 0, 0); SB(); s_ex(2, 0, 0, 2); SB(); qk(6, 0, 1); SB(); k_rd(6, 5); SB();
  qk(6, 1, 0); SB(); s_ex(2, 0, 2, 4); SB(); qk(6, 1, 1); SB(); k_rd(6, 6); SB(); s_ex(2, 1, 0, 2); SB(); qk(6, 2, 0); SB(); s_ex(2, 1, 2, 4); SB();
  qk(6, 2, 1); SB(); k_rd(6, 7); SB(); p_pack(2, 0); SB(); qk(6, 3, 0); SB(); qk(6, 3, 1); SB(); p_pack(2, 1); SB();
  SYNC(63, 14);
  qk(6, 4, 0); SB(); p_wr(2); SB(); k_base(IC(14), 0); SB(); k_rd(7, 0); SB(); qk(6, 4, 1); SB(); k_base(IC(14), 1); SB(); qk(6, 5, 0); SB();
  k_rd(7, 1); SB(); rmax(5, 0); SB(); qk(6, 5, 1); SB(); k_base(IC(14), 2); SB(); k_rd(7, 2); SB(); rmax(5, 1); SB(); qk(6, 6, 0); SB();
  k_base(IC(14), 3); SB(); qk(6, 6, 1); SB(); k_rd(7, 3); SB(); s_fm(3, 0); SB(); qk(6, 7, 0); SB(); s_fm(3, 1); SB(); qk(6, 7, 1); SB();
  k_rd(7, 4); SB(); qk(7, 0, 0); SB(); s_ex(3, 0, 0, 2); SB(); qk(7, 0, 1); SB(); k_rd(7, 5); SB(); qk(7, 1, 0); SB(); s_ex(3, 0, 2, 4); SB();
  p_rd(0, 0, 0); SB(); p_rd(0, 0, 1); SB(); qk(7, 1, 1); SB(); k_rd(7, 6); SB(); s_ex(3, 1, 0, 2); SB(); p_rd(0, 1, 0); SB(); p_rd(0, 1, 1); SB();
  qk(7, 2, 0); SB(); s_ex(3, 1, 2, 4); SB(); qk(7, 2, 1); SB(); k_rd(7, 7); SB(); p_pack(3, 0); SB(); qk(7, 3, 0); SB(); qk(7, 3, 1); SB();
  p_pack(3, 1); SB();
  SYNC(63, 15);
  qk(7, 4, 0); SB(); tr_base(IC(15)); SB(); qk(7, 4, 1); SB(); v_rd(0, 0); SB(); qk(7, 5, 0); SB(); v_rd(0, 1); SB(); rmax(6, 0); SB();
  qk(7, 5, 1); SB(); v_rd(0, 2); SB(); rmax(6, 1); SB(); qk(7, 6, 0); SB(); v_rd(0, 3); SB(); qk(7, 6, 1); SB(); p_rd(0, 2, 0); SB();
  p_rd(0, 2, 1); SB(); qk(7, 7, 0); SB(); p_rd(0, 3, 0); SB(); p_rd(0, 3, 1); SB(); qk(7, 7, 1); SB(); pv(0, 0, 0); SB(); pv(0, 0, 1); SB();
  rmax(7, 0); SB(); rmax(7, 1); SB(); pv(0, 1, 0); SB(); m_publish(); SB(); pv(0, 1, 1); SB(); pv(0, 2, 0); SB(); pv(0, 2, 1); SB();
  SYNC(63, 16);
  pv(0, 3, 0); SB(); m_read(); m_read2(); SB(); tr_base(IC(16)); SB(); v_rd(1, 0); SB(); p_rd(1, 0, 0); SB(); p_rd(1, 0, 1); SB(); v_rd(1, 1); SB();
  p_rd(1, 1, 0); SB(); p_rd(1, 1, 1); SB(); pv(0, 3, 1); SB(); v_rd(1, 2); SB(); p_rd(1, 2, 0); SB(); p_rd(1, 2, 1); SB(); v_rd(1, 3); SB();
  p_rd(1, 3, 0); SB(); p_rd(1, 3, 1); SB(); pv(1, 0, 0); SB(); m_fin2(); m_fin2d(); SB(); pv(1, 0, 1); SB(); s_fm(4, 0); SB(); s_fm(4, 1); SB();
  pv(1, 1, 0); SB(); s_ex(4, 0, 0, 2); SB(); s_ex(4, 0, 2, 4); SB(); pv(1, 1, 1); SB(); s_ex(4, 1, 0, 2); SB(); pv(1, 2, 0); SB();
  s_ex(4, 1, 2, 4); SB(); p_pack(4, 0); SB(); pv(1, 2, 1); SB(); p_pack(4, 1); SB();
  SYNC(63, 17);
  pv(1, 3, 0); SB(); p_wr(3); SB(); p_wr(4); SB(); tr_base(IC(17)); SB(); v_rd(2, 0); SB(); p_rd(2, 0, 0); SB(); p_rd(2, 0, 1); SB(); v_rd(2, 1); SB();
  p_rd(2, 1, 0); SB(); p_rd(2, 1, 1); SB(); s_fm(5, 0); SB(); pv(1, 3, 1); SB(); v_rd(2, 2); SB(); p_rd(2, 2, 0); SB(); p_rd(2, 2, 1); SB();
  v_rd(2, 3); SB(); p_rd(2, 3, 0); SB(); p_rd(2, 3, 1); SB(); s_fm(5, 1); SB(); pv(2, 0, 0); SB(); s_ex(5, 0, 0, 2); SB(); pv(2, 0, 1); SB();
  pv(2, 1, 0); SB(); pv(2, 1, 1); SB(); s_ex(5, 0, 2, 4); SB(); s_ex(5, 1, 0, 2); SB(); pv(2, 2, 0); SB(); s_ex(5, 1, 2, 4); SB(); p_pack(5, 0); SB();
  pv(2, 2, 1); SB(); p_pack(5, 1); SB();
  SYNC(63, 18);
  pv(2, 3, 0); SB(); p_wr(5); SB(); tr_base(IC(18)); SB(); v_rd(3, 0); SB(); p_rd(3, 0, 0); SB(); p_rd(3, 0, 1); SB(); v_rd(3, 1); SB();
  p_rd(3, 1, 0); SB(); p_rd(3, 1, 1); SB(); s_fm(6, 0); SB(); pv(2, 3, 1); SB(); v_rd(3, 2); SB(); p_rd(3, 2, 0); SB(); p_rd(3, 2, 1); SB();
  v_rd(3, 3); SB(); p_rd(3, 3, 0); SB(); p_rd(3, 3, 1); SB(); s_fm(6, 1); SB(); pv(3, 0, 0); SB(); s_ex(6, 0, 0, 2); SB(); pv(3, 0, 1); SB();
  s_ex(6, 0, 2, 4); SB(); pv(3, 1, 0); SB(); pv(3, 1, 1); SB(); s_ex(6, 1, 0, 2); SB(); p_rd(4, 0, 0); SB(); p_rd(4, 0, 1); SB(); pv(3, 2, 0); SB();
  s_ex(6, 1, 2, 4); SB(); p_rd(4, 1, 0); SB(); p_rd(4, 1, 1); SB(); pv(3, 2, 1); SB(); p_pack(6, 0); SB(); p_rd(4, 2, 0); SB(); p_rd(4, 2, 1); SB();
  pv(3, 3, 0); SB(); p_pack(6, 1); SB(); p_rd(4, 3, 0); SB(); p_rd(4, 3, 1); SB(); pv(3, 3, 1); SB();
  SYNC(63, 19);
  tr_base(IC(19)); SB(); v_rd(4, 0); SB(); v_rd(4, 1); SB(); v_rd(4, 2); SB(); v_rd(4, 3); SB(); p_wr(6); SB(); resc(0); SB(); pv(4, 0, 0); SB();
  s_fm(7, 0); SB(); pv(4, 1, 0); SB(); s_fm(7, 1); SB(); pv(4, 2, 0); SB(); s_ex(7, 0, 0, 2); SB(); pv(4, 3, 0); SB(); resc(1); SB();
  p_rd(5, 0, 0); SB(); p_rd(5, 0, 1); SB(); pv(4, 0, 1); SB(); p_rd(5, 1, 0); SB(); p_rd(5, 1, 1); SB(); pv(4, 1, 1); SB(); s_ex(7, 0, 2, 4); SB();
  s_ex(7, 1, 0, 2); SB(); p_rd(5, 2, 0); SB(); p_rd(5, 2, 1); SB(); pv(4, 2, 1); SB(); s_ex(7, 1, 2, 4); SB(); p_pack(7, 0); SB(); p_rd(5, 3, 0); SB();
  p_rd(5, 3, 1); SB(); pv(4, 3, 1); SB(); p_pack(7, 1); SB();
  SYNC(63, 20);
  tr_base(IC(20)); SB(); v_rd(5, 0); SB(); v_rd(5, 1); SB(); v_rd(5, 2); SB(); v_rd(5, 3); SB(); p_wr(7); SB(); pv(5, 0, 0); SB(); pv(5, 0, 1); SB();
  pv(5, 1, 0); SB(); pv(5, 1, 1); SB(); p_rd(6, 0, 0); SB(); p_rd(6, 0, 1); SB(); pv(5, 2, 0); SB(); l_publish(); SB(); p_rd(6, 1, 0); SB();
  p_rd(6, 1, 1); SB(); pv(5, 2, 1); SB(); lanesE(); SB(); p_rd(6, 2, 0); SB(); p_rd(6, 2, 1); SB(); pv(5, 3, 0); SB(); p_rd(6, 3, 0); SB();
  p_rd(6, 3, 1); SB(); pv(5, 3, 1); SB();
  SYNC(63, 21);
  tr_base(IC(21)); SB(); v_rd(6, 0); SB(); v_rd(6, 1); SB(); v_rd(6, 2); SB(); v_rd(6, 3); SB(); pv(6, 0, 0); SB(); pv(6, 0, 1); SB();
  pv(6, 1, 0); SB(); pv(6, 1, 1); SB(); p_rd(7, 0, 0); SB(); p_rd(7, 0, 1); SB(); pv(6, 2, 0); SB(); p_rd(7, 1, 0); SB(); p_rd(7, 1, 1); SB();
  pv(6, 2, 1); SB(); p_rd(7, 2, 0); SB(); p_rd(7, 2, 1); SB(); pv(6, 3, 0); SB(); p_rd(7, 3, 0); SB(); p_rd(7, 3, 1); SB(); pv(6, 3, 1); SB();
  SYNC(63, 32);
  tr_base(IC(22)); SB(); v_rd(7, 0); SB(); v_rd(7, 1); SB(); l_read(); SB(); pv(7, 0, 0); SB(); pv(7, 0, 1); SB(); l_finish(); SB(); pv(7, 1, 0); SB();
  pv(7, 1, 1); SB();
  SYNC(63, 22);
  v_rd(7, 2); SB(); v_rd(7, 3); SB(); pv(7, 2, 0); SB(); pv(7, 3, 0); SB(); pv(7, 2, 1); SB(); pv(7, 3, 1); SB(); img_wr_o(0, 0); SB();
  img_wr_o(0, 1); SB(); img_wr_o(0, 2); SB(); img_wr_o(0, 3); SB(); img_wr_o(1, 0); SB(); img_wr_o(1, 1); SB(); img_wr_o(1, 2); SB();
  img_wr_o(1, 3); SB();
  SYNC(63, 23);
  wo_base(IC(23)); SB(); wo_rd(0, 0); SB(); o_rd(0, 0); SB(); o_rd(1, 0); SB(); wo_rd(0, 1); SB(); o_rd(0, 1); SB(); o_rd(1, 1); SB();
  wo_rd(0, 2); SB(); o_rd(0, 2); SB(); o_rd(1, 2); SB(); wo_rd(0, 3); SB(); o_rd(0, 3); SB(); o_rd(1, 3); SB(); wo_rd(0, 4); SB(); o_rd(0, 4); SB();
  o_rd(1, 4); SB(); wo_rd(0, 5); SB(); o_rd(0, 5); SB(); o_rd(1, 5); SB(); wo_rd(0, 6); SB(); o_rd(0, 6); SB(); o_rd(1, 6); SB(); wo_rd(0, 7); SB();
  o_rd(0, 7); SB(); o_rd(1, 7); SB(); em(0, 0, 0); SB(); em(0, 0, 1); SB(); em(0, 1, 0); SB(); em(0, 1, 1); SB(); em(0, 2, 0); SB(); em(0, 2, 1); SB();
  em(0, 3, 0); SB(); em(0, 3, 1); SB(); em(0, 4, 0); SB(); em(0, 4, 1); SB();
  SYNC(63, 24);
  em(0, 5, 0); SB(); wo_base(IC(24)); SB(); wo_rd(1, 0); SB(); wo_rd(1, 1); SB(); em(0, 5, 1); SB(); wo_rd(1, 2); SB(); em(0, 6, 0); SB();
  wo_rd(1, 3); SB(); em(0, 6, 1); SB(); wo_rd(1, 4); SB(); em(0, 7, 0); SB(); em(0, 7, 1); SB(); wo_rd(1, 5); SB(); em(1, 0, 0); SB();
  em(1, 0, 1); SB(); wo_rd(1, 6); SB(); e_pack(0, 0); SB(); em(1, 1, 0); SB(); e_pack(0, 1); SB(); em(1, 1, 1); SB(); wo_rd(1, 7); SB();
  e_store(0); SB(); em(1, 2, 0); SB(); em(1, 2, 1); SB(); em(1, 3, 0); SB(); em(1, 3, 1); SB(); em(1, 4, 0); SB(); em(1, 4, 1); SB();
  SYNC(63, 25);
  em(1, 5, 0); SB(); wo_base(IC(25)); SB(); wo_rd(2, 0); SB(); wo_rd(2, 1); SB(); em(1, 5, 1); SB(); wo_rd(2, 2); SB(); em(1, 6, 0); SB();
  wo_rd(2, 3); SB(); em(1, 6, 1); SB(); wo_rd(2, 4); SB(); em(1, 7, 0); SB(); em(1, 7, 1); SB(); wo_rd(2, 5); SB(); em(2, 0, 0); SB();
  em(2, 0, 1); SB(); wo_rd(2, 6); SB(); e_pack(1, 0); SB(); em(2, 1, 0); SB(); e_pack(1, 1); SB(); em(2, 1, 1); SB(); wo_rd(2, 7); SB();
  e_store(1); SB(); em(2, 2, 0); SB(); em(2, 2, 1); SB(); em(2, 3, 0); SB(); em(2, 3, 1); SB(); em(2, 4, 0); SB(); em(2, 4, 1); SB();
  SYNC(63, 26);
  em(2, 5, 0); SB(); wo_base(IC(26)); SB(); wo_rd(3, 0); SB(); wo_rd(3, 1); SB(); em(2, 5, 1); SB(); wo_rd(3, 2); SB(); em(2, 6, 0); SB();
  wo_rd(3, 3); SB(); em(2, 6, 1); SB(); wo_rd(3, 4); SB(); em(2, 7, 0); SB(); em(2, 7, 1); SB(); wo_rd(3, 5); SB(); em(3, 0, 0); SB();
  em(3, 0, 1); SB(); wo_rd(3, 6); SB(); e_pack(2, 0); SB(); em(3, 1, 0); SB(); e_pack(2, 1); SB(); em(3, 1, 1); SB(); wo_rd(3, 7); SB();
  e_store(2); SB(); em(3, 2, 0); SB(); em(3, 2, 1); SB(); em(3, 3, 0); SB(); em(3, 3, 1); SB(); em(3, 4, 0); SB(); em(3, 4, 1); SB();
  SYNC(63, 27);
  em(3, 5, 0); SB(); wo_base(IC(27)); SB(); wo_rd(4, 0); SB(); wo_rd(4, 1); SB(); em(3, 5, 1); SB(); wo_rd(4, 2); SB(); em(3, 6, 0); SB();
  wo_rd(4, 3); SB(); em(3, 6, 1); SB(); wo_rd(4, 4); SB(); em(3, 7, 0); SB(); em(3, 7, 1); SB(); wo_rd(4, 5); SB(); em(4, 0, 0); SB();
  em(4, 0, 1); SB(); wo_rd(4, 6); SB(); e_pack(3, 0); SB(); em(4, 1, 0); SB(); e_pack(3, 1); SB(); em(4, 1, 1); SB(); wo_rd(4, 7); SB();
  e_store(3); SB(); em(4, 2, 0); SB(); em(4, 2, 1); SB(); em(4, 3, 0); SB(); em(4, 3, 1); SB(); em(4, 4, 0); SB(); em(4, 4, 1); SB();
  SYNC(63, 28);
  em(4, 5, 0); SB(); wo_base(IC(28)); SB(); wo_rd(5, 0); SB(); wo_rd(5, 1); SB(); em(4, 5, 1); SB(); wo_rd(5, 2); SB(); em(4, 6, 0); SB();
  wo_rd(5, 3); SB(); em(4, 6, 1); SB(); wo_rd(5, 4); SB(); em(4, 7, 0); SB(); em(4, 7, 1); SB(); wo_rd(5, 5); SB(); em(5, 0, 0); SB();
  em(5, 0, 1); SB(); wo_rd(5, 6); SB(); e_pack(4, 0); SB(); em(5, 1, 0); SB(); e_pack(4, 1); SB(); em(5, 1, 1); SB(); wo_rd(5, 7); SB();
  e_store(4); SB(); em(5, 2, 0); SB(); em(5, 2, 1); SB(); em(5, 3, 0); SB(); em(5, 3, 1); SB(); em(5, 4, 0); SB(); em(5, 4, 1); SB();
  SYNC(0, 29);
  em(5, 5, 0); SB(); wo_base(IC(29)); SB(); e_flag(0, 5); SB(); wo_rd(6, 0); SB(); wo_rd(6, 1); SB(); em(5, 5, 1); SB(); wo_rd(6, 2); SB();
  em(5, 6, 0); SB(); wo_rd(6, 3); SB(); em(5, 6, 1); SB(); wo_rd(6, 4); SB(); em(5, 7, 0); SB(); em(5, 7, 1); SB(); wo_rd(6, 5); SB();
  em(6, 0, 0); SB(); em(6, 0, 1); SB(); wo_rd(6, 6); SB(); e_pack(5, 0); SB(); em(6, 1, 0); SB(); e_pack(5, 1); SB(); em(6, 1, 1); SB();
  wo_rd(6, 7); SB(); e_store(5); SB(); em(6, 2, 0); SB(); em(6, 2, 1); SB(); em(6, 3, 0); SB(); em(6, 3, 1); SB(); em(6, 4, 0); SB();
  em(6, 4, 1); SB(); em(6, 5, 0); SB(); em(6, 5, 1); SB(); em(6, 6, 0); SB(); em(6, 6, 1); SB(); em(6, 7, 0); SB(); em(6, 7, 1); SB(); v_clear(); SB();
  e_pack(6, 0); SB(); e_pack(6, 1); SB(); e_store(6); SB(); poll0(); SB(); vmwait<0>(); bar(); e_flag(5, 7); SB();
  if (w < 7) {
    for (int spins = 0; spins < (1 << 16); ++spins) {
      const unsigned ok = (unsigned)__ballot(fv == tag) | (1u << h);
      const unsigned rdy = ok & (ok >> 1) & 0x5555u & ~done;
      asm volatile("" ::: "memory");
#pragma unroll
      for (int u = 0; u < 8; ++u)
        if ((rdy >> (2 * u)) & 1) v[u] = __builtin_amdgcn_raw_buffer_load_b128(rsP, (2 * u + hh == h) ? 0x7ff00000 : r32 * 16 + (2 * u + hh) * PL_H, h * PL_C + ct * PL_T, AUX_SC1_NT);
      done |= rdy;
      if (done == 0x5555u) break;
      __builtin_amdgcn_s_sleep(1);
      fv = __hip_atomic_load(fp, RLX_AGENT);
    }
    if (h == 0 && w == 0 && lb == 0) __hip_atomic_store((GAS u64*)(part + (size_t)b * PART_STRIDE + EPOCH_OFF), tag, RLX_AGENT);
    float sm[8];
    {
      u32x4 own = *(LAS u32x4*)(lds + ct * 512 + r32 * 16);
      if (hh != (h & 1)) own = u32x4{0u, 0u, 0u, 0u};
#pragma unroll
      for (int e = 0; e < 4; ++e) { sm[2 * e] = __uint_as_float(own[e] << 16); sm[2 * e + 1] = __uint_as_float(own[e] & 0xffff0000u); }
    }
#pragma unroll
    for (int u = 0; u < 8; ++u)
#pragma unroll
      for (int e = 0; e < 4; ++e) { sm[2 * e] += __uint_as_float(v[u][e] << 16); sm[2 * e + 1] += __uint_as_float(v[u][e] & 0xffff0000u); }
    float o[4];
#pragma unroll
    for (int e = 0; e < 4; ++e) {
      auto x = __builtin_amdgcn_permlane32_swap(__float_as_uint(sm[e]), __float_as_uint(sm[4 + e]), false, false);
      o[e] = __uint_as_float(x[0]) + __uint_as_float(x[1]);
    }
    int T = ct + rot; if (T >= 7) T -= 7;
    const int q = 4 * h + 2 * (r32 & 1) + hh, n = 64 * T + 4 * (r32 >> 1);
    u32x2 r = {pk2(o[0], o[1]), pk2(o[2], o[3])};
    auto rsOut = __builtin_amdgcn_make_buffer_rsrc((void*)(out + (size_t)b * (SQ * DM * 2)), 0, SQ * DM * 2, 0x00020000);
    __builtin_amdgcn_raw_buffer_store_b64(r, rsOut, (q * DM + n) * 2, 0, AUX_SC1);
  }
}

extern "C" void kernel_launch(void* const* d_in, const int* in_sizes, int n_in, void* d_out, int out_size, void* d_ws, size_t ws_size,
                              hipStream_t stream) {
  (void)hipFuncSetAttribute((const void*)gqa_main, hipFuncAttributeMaxDynamicSharedMemorySize, LDS_BYTES);
  gqa_main<<<256, 768, LDS_BYTES, stream>>>((const char*)d_in[0], (const char*)d_in[1], (const char*)d_in[2], (const char*)d_in[3],
                                            (const char*)d_in[4], (char*)d_ws, (char*)d_out);
}
```
